```python
import torch

def _softmax(x, axis=-1):
    return torch.softmax(x, dim=axis)
def _gelu(x):
    return torch.nn.functional.gelu(x, approximate="tanh")
def _silu(x):
    return torch.nn.functional.silu(x)
def _sigmoid(x):
    return torch.sigmoid(x)
def _layernorm(x, axis=-1, eps=1e-5):
    m = x.mean(dim=axis, keepdim=True)
    v = x.var(dim=axis, keepdim=True, unbiased=False)
    return (x - m) / torch.sqrt(v + eps)
def _scatter_add(tgt, idx, src, axis=0):
    return tgt.clone().index_add_(axis, idx, src)
def _segment_sum(src, seg, n):
    out = torch.zeros((n,) + src.shape[1:], dtype=src.dtype)
    return out.index_add_(0, seg, src)
def _pad(x, pw):
    flat = tuple(v for pair in reversed(pw) for v in pair)
    return torch.nn.functional.pad(x, flat)

kernel_name = 'gated_mix__linear_attn_block__sliding_local_dilated__prompt'

def setup_inputs():

    _w = (lambda a: (lambda t: t.to(torch.bfloat16) if t.is_floating_point() else t)(torch.as_tensor(a)))
    def _A_setup():
        B, S, D = 1, 2048, 512
        return {
            "x": _w((lambda *s: torch.randn(*s, dtype=torch.bfloat16) * 0.05)(B, S, D)),
            "wqkv": _w((lambda *s: torch.randn(*s, dtype=torch.bfloat16) * 1.6)(D, 3 * D)),
            "wo": _w((lambda *s: torch.randn(*s, dtype=torch.bfloat16) * 0.4)(D, D)),
        }
    _A_in = _A_setup()

    def _B_setup():
        B, S, D = 1, 2048, 512
        return {
            "x": _w((lambda *s: torch.randn(*s, dtype=torch.bfloat16) * 0.05)(B, S, D)),
            "wqkv": _w((lambda *s: torch.randn(*s, dtype=torch.bfloat16) * 0.8)(D, 3 * D)),
            "wo": _w((lambda *s: torch.randn(*s, dtype=torch.bfloat16) * 0.05)(D, D)),
        }
    _B_in = _B_setup()

    return {
        "A_x": _A_in["x"],
        "A_wqkv": _A_in["wqkv"],
        "A_wo": _A_in["wo"],
        "B_x": _B_in["x"],
        "B_wqkv": _B_in["wqkv"],
        "B_wo": _B_in["wo"],
        "gate_w": _w((lambda *s: torch.randn(*s, dtype=torch.bfloat16) * 0.05)(512) * 16.0),
    }

def reference(A_x, A_wqkv, A_wo, B_x, B_wqkv, B_wo, gate_w):
    def _A_ref(x, wqkv, wo):
        B, S, D = x.shape
        H = 32; Dh = D // H
        qkv = (lambda x, *d: x.permute(*d))((x @ wqkv).reshape(B, S, 3, H, Dh), 2, 0, 3, 1, 4)
        q, k, v = qkv[0], qkv[1], qkv[2]
        phi = lambda t: torch.where(t > 0, t + 1, torch.exp(t)) + 1e-6
        pq, pk = phi(q), phi(k)
        kv = torch.einsum("bhsd,bhse->bhde", pk, v)
        z = torch.einsum("bhsd->bhd", pk)
        num = torch.einsum("bhqd,bhde->bhqe", pq, kv)
        den = torch.einsum("bhqd,bhd->bhq", pq, z)[..., None] + 1e-6
        o = num / den
        return (lambda x, *d: x.permute(*d))(o, 0, 2, 1, 3).reshape(B, S, D) @ wo

    def _B_ref(x, wqkv, wo):
        B, S, D = x.shape
        H = 32; Dh = D // H
        W, dil = 8, 2
        qkv = (lambda x, *d: x.permute(*d))((x @ wqkv).reshape(B, S, 3, H, Dh), 2, 0, 3, 1, 4)
        q, k, v = qkv[0], qkv[1], qkv[2]
        scale = Dh ** -0.5
        pos = torch.arange(S)
        off = torch.arange(W) * dil
        idx = pos[:, None] - off[None, :]
        mask = idx >= 0
        idx_c = torch.where(mask, idx, 0).reshape(-1)
        kg = k[:, :, idx_c, :].reshape(B, H, S, W, Dh)
        vg = v[:, :, idx_c, :].reshape(B, H, S, W, Dh)
        logits = torch.einsum("bhsd,bhswd->bhsw", q, kg) * scale
        logits = torch.where(mask[None, None, :, :], logits, -1e9)
        attn = _softmax(logits, axis=-1)
        o = torch.einsum("bhsw,bhswd->bhsd", attn, vg)
        return (lambda x, *d: x.permute(*d))(o, 0, 2, 1, 3).reshape(B, S, D) @ wo

    x = A_x
    g = _sigmoid((x * gate_w).sum(-1, keepdims=True))
    a = _A_ref(x, A_wqkv, A_wo)
    b = _B_ref(x, B_wqkv, B_wo)
    return g * a + (1 - g) * b
```

```cpp
#include <hip/hip_runtime.h>
#include <hip/hip_bf16.h>
#include <type_traits>
#define PF_CODE 0
#define PF_TOUCH 0

typedef unsigned short u16;
typedef short s16x4 __attribute__((ext_vector_type(4)));
typedef __bf16 bf16x8 __attribute__((ext_vector_type(8)));
typedef __bf16 bf16x2 __attribute__((ext_vector_type(2)));
typedef float f32x4 __attribute__((ext_vector_type(4)));
typedef float f32x2 __attribute__((ext_vector_type(2)));
typedef unsigned u32x4 __attribute__((ext_vector_type(4)));
typedef unsigned u32x2 __attribute__((ext_vector_type(2)));
#define LAS __attribute__((address_space(3)))
#define GAS __attribute__((address_space(1)))
#define DI __device__ __forceinline__
typedef GAS unsigned gu32;
#define RLX_AGENT __ATOMIC_RELAXED, __HIP_MEMORY_SCOPE_AGENT
template <int V> using IC = std::integral_constant<int, V>;

constexpr int STAGE = 43008;
constexpr int XB = 18432;
constexpr int WO_A = 0;
constexpr int WO_B = 86016;
constexpr int WS_EPOCH = 0;
constexpr int WS_F1 = 4096;
constexpr int WS_F2 = WS_F1 + 512 * 128;
constexpr int WS_G = WS_F2 + 256 * 128;
constexpr int WS_P1 = WS_G + 8192;
constexpr int P1_STRIDE = 1152;
constexpr int WS_ACT = 1 << 20;
constexpr int WS_STAMP = 8 << 20;
constexpr unsigned SPIN_CAP = 1u << 18;

DI void glds(const void* sbase, unsigned voff, unsigned ldsaddr) {
    asm volatile("s_mov_b32 m0, %2\n\ts_nop 0\n\tglobal_load_lds_dwordx4 %0, %1" :: "v"(voff), "s"(sbase), "s"(ldsaddr) : "memory");
}
DI void glds_half(const void* sbase, unsigned voff, unsigned ldsaddr) {
    asm volatile("s_mov_b32 m0, %2\n\ts_mov_b64 exec, 0xffffffff\n\tglobal_load_lds_dwordx4 %0, %1\n\ts_mov_b64 exec, -1" :: "v"(voff), "s"(sbase), "s"(ldsaddr) : "memory");
}
template <int N> DI void waitv() { asm volatile("s_waitcnt vmcnt(%0)" :: "n"(N) : "memory"); }
DI void waitl0() { asm volatile("s_waitcnt lgkmcnt(0)" ::: "memory"); }
#define BAR() __builtin_amdgcn_s_barrier()
#define SB() __builtin_amdgcn_sched_barrier(0)

DI bf16x8 cat8(s16x4 lo, s16x4 hi) { return __builtin_bit_cast(bf16x8, __builtin_shufflevector(lo, hi, 0, 1, 2, 3, 4, 5, 6, 7)); }
DI unsigned pk2(float a, float b) { bf16x2 h = {(__bf16)a, (__bf16)b}; return __builtin_bit_cast(unsigned, h); }
DI s16x4 pk4(f32x4 v) { u32x2 p = {pk2(v[0], v[1]), pk2(v[2], v[3])}; return __builtin_bit_cast(s16x4, p); }
DI f32x4 mfma32(bf16x8 a, bf16x8 b, f32x4 c) { return __builtin_amdgcn_mfma_f32_16x16x32_bf16(a, b, c, 0, 0, 0); }
DI f32x4 mfma16(s16x4 a, s16x4 b, f32x4 c) { return __builtin_amdgcn_mfma_f32_16x16x16bf16_1k(a, b, c, 0, 0, 0); }
DI s16x4 trread(unsigned addr) { return __builtin_amdgcn_ds_read_tr16_b64_v4i16((LAS s16x4*)(size_t)addr); }
DI float fmx(float a, float b) { return __builtin_elementwise_maximum(a, b); }
DI unsigned phi2(float a, float b) {
    f32x2 y = (f32x2){a, b} * 1.4426950408889634f;
    f32x2 e = {__builtin_amdgcn_fmed3f(__builtin_amdgcn_exp2f(y[0]), 0.f, 1.f), __builtin_amdgcn_fmed3f(__builtin_amdgcn_exp2f(y[1]), 0.f, 1.f)};
    f32x2 m = {__int_as_float(max(__float_as_int(a), 0)), __int_as_float(max(__float_as_int(b), 0))};
    f32x2 r = (e + 1e-6f) + m;
    return pk2(r[0], r[1]);
}
DI s16x4 phi4(f32x4 t) { return __builtin_bit_cast(s16x4, (u32x2){phi2(t[0], t[1]), phi2(t[2], t[3])}); }
template <int CTRL> DI float dpp_add(float v) { return v + __builtin_bit_cast(float, __builtin_amdgcn_mov_dpp(__builtin_bit_cast(int, v), CTRL, 0xf, 0xf, true)); }
DI float row_sum16(float v) { v = dpp_add<0x128>(v); v = dpp_add<0x124>(v); v = dpp_add<0x122>(v); return dpp_add<0x121>(v); }
DI void xrow_max4(float& v0, float& v1, float& v2, float& v3) {
    auto a = __builtin_amdgcn_permlane32_swap(__float_as_uint(v0), __float_as_uint(v1), false, false);
    auto b = __builtin_amdgcn_permlane32_swap(__float_as_uint(v2), __float_as_uint(v3), false, false);
    const float c = fmx(__uint_as_float(a[0]), __uint_as_float(a[1]));
    const float d = fmx(__uint_as_float(b[0]), __uint_as_float(b[1]));
    auto e = __builtin_amdgcn_permlane16_swap(__float_as_uint(c), __float_as_uint(d), false, false);
    const unsigned f = __float_as_uint(fmx(__uint_as_float(e[0]), __uint_as_float(e[1])));
    auto h = __builtin_amdgcn_permlane16_swap(f, f, false, false);
    auto p = __builtin_amdgcn_permlane32_swap(h[0], h[0], false, false);
    auto q = __builtin_amdgcn_permlane32_swap(h[1], h[1], false, false);
    v0 = __uint_as_float(p[0]); v1 = __uint_as_float(p[1]); v2 = __uint_as_float(q[0]); v3 = __uint_as_float(q[1]);
}
DI float xrow_sum(float v) {
    auto a = __builtin_amdgcn_permlane32_swap(__float_as_uint(v), __float_as_uint(v), false, false);
    v = __uint_as_float(a[0]) + __uint_as_float(a[1]);
    auto b = __builtin_amdgcn_permlane16_swap(__float_as_uint(v), __float_as_uint(v), false, false);
    return __uint_as_float(b[0]) + __uint_as_float(b[1]);
}
DI u32x4 pair16(s16x4 t0, s16x4 t1) {
    u32x2 a = __builtin_bit_cast(u32x2, t0), b = __builtin_bit_cast(u32x2, t1);
    auto lo = __builtin_amdgcn_permlane16_swap(a[0], b[0], false, false);
    auto hi = __builtin_amdgcn_permlane16_swap(a[1], b[1], false, false);
    return (u32x4){lo[0], hi[0], lo[1], hi[1]};
}
DI void wait_flags(const unsigned* base, int stride, int n, unsigned tag, int lane) {
    const gu32* p = (const gu32*)base + (lane < n ? lane : 0) * stride;
    for (unsigned spins = 0; spins < SPIN_CAP; ++spins) {
        unsigned f = __hip_atomic_load(p, RLX_AGENT);
        if (__all(f == tag)) break;
        __builtin_amdgcn_s_sleep(1);
    }
    __builtin_amdgcn_fence(__ATOMIC_ACQUIRE, "wavefront");
}
#define STAMP(i) do {} while (0)

template <int NM> struct Frags { bf16x8 x[NM]; s16x4 wl[3], wh[3]; };

template <bool TB>
DI void loader(const u16* x, const u16* W, const u16* woa, const u16* wob, const u16* gw, char* ws, unsigned lds0,
               int r, int Q, int c2, int w, int lane, int wave, unsigned long long& tprobe) {
    constexpr int NG = TB ? 11 : 10;
    const int row8 = lane >> 3, pos = lane & 7;
    const int row0 = r * 128;
    const int grow = row0 + ((TB ? 8 : 0) + Q) * 8 + 2 * w;
    u32x4 gx0, gx1, gwv;
    {
        unsigned vo = (unsigned)(grow * 1024 + lane * 16), vg = (unsigned)(lane * 16);
        asm volatile("global_load_dwordx4 %0, %1, %2" : "=v"(gx0) : "v"(vo), "s"(x) : "memory");
        asm volatile("global_load_dwordx4 %0, %1, %2 offset:1024" : "=v"(gx1) : "v"(vo), "s"(x) : "memory");
        asm volatile("global_load_dwordx4 %0, %1, %2" : "=v"(gwv) : "v"(vg), "s"(gw) : "memory");
    }
    const int xsw = ((w & 1) << 2) | (row8 >> 1);
    unsigned vx[4], vxh = 0, vw[6];
    #pragma unroll
    for (int q = 0; q < 4; ++q) vx[q] = (unsigned)((row0 + (w + 4 * q) * 8 + row8) * 1024 + ((pos ^ xsw) << 4));
    if (TB) { int hr = 4 * w + (row8 & 3); int gr = row0 - 16 + hr; if (gr < 0) gr += 16; vxh = (unsigned)(gr * 1024 + ((pos ^ ((hr >> 1) & 7)) << 4)); }
    const int wf = ((row8 >> 1) & 1) | ((w & 1) << 1);
    const int wch = (((pos >> 1) ^ wf) << 1) | (pos & 1);
    #pragma unroll
    for (int i = 0; i < 6; ++i) { int ii = w + 4 * i; vw[i] = (unsigned)(((ii & 7) * 8 + row8) * 3072 + (ii >> 3) * 1024 + Q * 128 + wch * 16); }
    auto issue = [&](int g, int buf, int n) {
        const unsigned lb = lds0 + buf * STAGE;
        if (n < 6) glds((const char*)W + (size_t)g * 64 * 3072, vw[n], lb + XB + (w + 4 * n) * 1024);
        else if (n < 10) glds((const char*)x + g * 128, vx[n - 6], lb + 2048 + (w + 4 * (n - 6)) * 1024);
        else glds_half((const char*)x + g * 128, vxh, lb + w * 512);
    };
    auto issue_group = [&](int g, int buf) {
        #pragma unroll
        for (int n = 0; n < NG; ++n) issue(g, buf, n);
    };
    const unsigned vwo = (unsigned)((w * 8 + row8) * 1024 + c2 * 128 + wch * 16);
    auto issue_wo = [&](const u16* wo, int ldsoff) {
        #pragma unroll
        for (int i = 0; i < 16; ++i) glds((const char*)wo + i * 32768, vwo, lds0 + ldsoff + (w + 4 * i) * 1024);
    };
    issue_group(0, 0);
    #pragma unroll
    for (int n = 0; n < 5; ++n) issue(1, 1, n);
    waitv<5>();
    BAR();
    STAMP(1);
    #pragma unroll
    for (int n = 5; n < NG; ++n) issue(1, 1, n);
    issue_group(2, 2);
    float gsel;
    {
        asm volatile("s_waitcnt vmcnt(%3)" : "+v"(gx0), "+v"(gx1), "+v"(gwv) : "n"(2 * NG) : "memory");
        float d0 = 0.f, d1 = 0.f;
        #pragma unroll
        for (int i = 0; i < 4; ++i) {
            float wl = __uint_as_float(gwv[i] << 16), wh = __uint_as_float(gwv[i] & 0xffff0000u);
            d0 += __uint_as_float(gx0[i] << 16) * wl + __uint_as_float(gx0[i] & 0xffff0000u) * wh;
            d1 += __uint_as_float(gx1[i] << 16) * wl + __uint_as_float(gx1[i] & 0xffff0000u) * wh;
        }
        d0 = xrow_sum(row_sum16(d0)); d1 = xrow_sum(row_sum16(d1));
        float d = lane == 0 ? d0 : d1;
        gsel = __builtin_amdgcn_rcpf(1.f + __builtin_amdgcn_exp2f(-1.4426950408889634f * d));
    }
    int buf = 0;
    #pragma unroll 1
    for (int i = 0; i < 5; ++i) {
        waitv<NG>();
        BAR();
        issue_group(i + 3, buf);
        buf = buf == 2 ? 0 : buf + 1;
    }
    waitv<NG>(); BAR();
    issue_wo(wob, WO_B);
    waitv<16>(); BAR();
    BAR();
    issue_wo(woa, WO_A);
    if (lane < 2) __hip_atomic_store((gu32*)(ws + WS_G) + grow + lane, __float_as_uint(gsel), RLX_AGENT);
    waitv<0>();
    STAMP(2);
}

template <bool TB>
DI void matrix(u16* out, char* ws, unsigned lds0, int r, int Q, int rh2, int c2, int w, int lane, int wave, unsigned long long& tprobe) {
    constexpr int NM = TB ? 9 : 8;
    constexpr int NMF = TB ? 26 : 24;
    constexpr int NRD = NM + 6;
    constexpr int RD0 = 2;
    const unsigned tag = __hip_atomic_load((gu32*)(ws + WS_EPOCH), RLX_AGENT) + 1u;
    const int g = lane >> 4, s = lane & 15;
    const unsigned ax0 = (unsigned)((TB ? 0 : 2048) + s * 128 + ((g ^ (s >> 1)) << 4));
    const int q4 = s >> 2, p4 = lane & 3;
    const int rf = ((q4 >> 1) & 1) | ((g & 1) << 1);
    const unsigned awl = (unsigned)((8 * g + q4) * 128 + p4 * 8);
    f32x4 accq[NM], acck[NM], accv[NM];
    #pragma unroll
    for (int i = 0; i < NM; ++i) { accq[i] = (f32x4){0, 0, 0, 0}; acck[i] = (f32x4){0, 0, 0, 0}; accv[i] = (f32x4){0, 0, 0, 0}; }
    Frags<NM> A, B;
    auto rd_n = [&](Frags<NM>& F, unsigned bwv, unsigned bxv, int n) {
        if (n < 0 || n >= NRD) return;
        if (n < 6) { s16x4 v = trread(bwv + (n >> 1) * 8192 + (n & 1) * 512); if (n & 1) F.wh[n >> 1] = v; else F.wl[n >> 1] = v; }
        else F.x[n - 6] = *(const LAS bf16x8*)(size_t)(bxv + (n - 6) * 2048);
    };
    auto mm = [&](const Frags<NM>& F, int n) {
        int mu, t;
        if (TB) { if (n < 2) { mu = 0; t = n + 1; } else { mu = 1 + (n - 2) / 3; t = (n - 2) % 3; } } else { mu = n / 3; t = n % 3; }
        if (t == 0) accq[mu] = mfma32(cat8(F.wl[0], F.wh[0]), F.x[mu], accq[mu]);
        else if (t == 1) { if (TB) acck[mu] = mfma32(cat8(F.wl[1], F.wh[1]), F.x[mu], acck[mu]); else acck[mu] = mfma32(F.x[mu], cat8(F.wl[1], F.wh[1]), acck[mu]); }
        else accv[mu] = mfma32(F.x[mu], cat8(F.wl[2], F.wh[2]), accv[mu]);
    };
    BAR();
    STAMP(1);
    unsigned bx0 = lds0 + ax0, bx1 = lds0 + (ax0 ^ 64), bw = lds0 + XB + awl + ((w ^ rf) << 5);
    #pragma unroll
    for (int n = 0; n < NRD; ++n) rd_n(A, bw, bx0, n);
    int buf = 0;
    auto stage = [&](auto last_c) {
        constexpr int LAST = decltype(last_c)::value;
        SB();
        #pragma unroll
        for (int n = 0; n < NMF; ++n) { mm(A, n); rd_n(B, bw + 4096, bx1, n - RD0); SB(); }
        const int nbuf = buf == 2 ? 0 : buf + 1;
        const unsigned d = (unsigned)((nbuf - buf) * STAGE);
        bx0 += d; bx1 += d; bw += d; buf = nbuf;
        waitl0();
        BAR();
        SB();
        #pragma unroll
        for (int n = 0; n < NMF; ++n) { mm(B, n); if (!LAST) rd_n(A, bw, bx0, n - RD0); SB(); }
    };
    #pragma unroll 1
    for (int i = 0; i < 7; ++i) stage(IC<0>{});
    stage(IC<1>{});
    STAMP(2);

    const __amdgpu_buffer_rsrc_t rws = __builtin_amdgcn_make_buffer_rsrc(ws, 0, WS_STAMP, 0x00020000);
    const int head = Q * 4 + w;
    s16x4 op[8];
    if (!TB) {
        f32x4 kv = {0, 0, 0, 0}, z = {0, 0, 0, 0};
        const bf16x8 ones = __builtin_bit_cast(bf16x8, (u32x4){0x3f803f80u, 0x3f803f80u, 0x3f803f80u, 0x3f803f80u});
        #pragma unroll
        for (int u = 0; u < 4; ++u) {
            bf16x8 pk = cat8(phi4(acck[2 * u]), phi4(acck[2 * u + 1]));
            bf16x8 vv = cat8(pk4(accv[2 * u]), pk4(accv[2 * u + 1]));
            kv = mfma32(pk, vv, kv);
            z = mfma32(pk, ones, z);
        }
        const int pbase = WS_P1 + (head * 16 + r) * P1_STRIDE;
        __builtin_amdgcn_raw_buffer_store_b128(__builtin_bit_cast(u32x4, kv), rws, lane * 16, pbase, 16);
        if (s == 0) __builtin_amdgcn_raw_buffer_store_b128(__builtin_bit_cast(u32x4, z), rws, 1024 + g * 16, pbase, 16);
        SB();
        s16x4 pq[8];
        #pragma unroll
        for (int t = 0; t < 4; ++t) { pq[t] = phi4(accq[t]); asm volatile("" : "+v"(pq[t])); }
        waitv<0>();
        if (lane == 0) __hip_atomic_store((gu32*)(ws + WS_F1) + (head * 16 + r) * 32, tag, RLX_AGENT);
        STAMP(3);
        #pragma unroll
        for (int t = 4; t < 8; ++t) { pq[t] = phi4(accq[t]); asm volatile("" : "+v"(pq[t])); }
        wait_flags((const unsigned*)(ws + WS_F1) + head * 16 * 32, 32, 16, tag, lane);
        STAMP(4);
        u32x4 part[16];
        const int pb0 = WS_P1 + head * 16 * P1_STRIDE;
        u32x4 zp = __builtin_amdgcn_raw_buffer_load_b128(rws, s * P1_STRIDE + 1024 + g * 16, pb0, 16);
        #pragma unroll
        for (int i = 0; i < 16; ++i) part[i] = __builtin_amdgcn_raw_buffer_load_b128(rws, lane * 16, pb0 + i * P1_STRIDE, 16);
        SB();
        f32x4 zt = __builtin_bit_cast(f32x4, zp);
        #pragma unroll
        for (int i = 0; i < 4; ++i) zt[i] = dpp_add<0x128>(zt[i]);
        #pragma unroll
        for (int i = 0; i < 4; ++i) zt[i] = dpp_add<0x124>(zt[i]);
        #pragma unroll
        for (int i = 0; i < 4; ++i) zt[i] = dpp_add<0x122>(zt[i]);
        #pragma unroll
        for (int i = 0; i < 4; ++i) zt[i] = dpp_add<0x121>(zt[i]);
        const s16x4 za = pk4(zt);
        f32x4 den[8];
        #pragma unroll
        for (int t = 0; t < 8; ++t) den[t] = mfma16(za, pq[t], (f32x4){0, 0, 0, 0});
        SB();
        f32x4 k0 = __builtin_bit_cast(f32x4, part[0]), k1 = __builtin_bit_cast(f32x4, part[1]);
        #pragma unroll
        for (int i = 2; i < 16; i += 2) { k0 += __builtin_bit_cast(f32x4, part[i]); k1 += __builtin_bit_cast(f32x4, part[i + 1]); }
        float inv[8];
        #pragma unroll
        for (int t = 0; t < 8; ++t) inv[t] = __builtin_amdgcn_rcpf(den[t][0] + 1e-6f);
        const s16x4 kva = pk4(k0 + k1);
        SB();
        f32x4 num[8];
        #pragma unroll
        for (int t = 0; t < 8; ++t) num[t] = mfma16(kva, pq[t], (f32x4){0, 0, 0, 0});
        SB();
        #pragma unroll
        for (int t = 0; t < 8; ++t) op[t] = pk4(num[t] * inv[t]);
    } else {
        const float NEG = -1e30f;
        f32x4 mprev, mcur;
        #pragma unroll
        for (int j = 0; j < 4; ++j) {
            const int dc = s - 4 * g - j;
            mcur[j] = (dc >= 0 && dc <= 14 && (dc & 1) == 0) ? 0.f : NEG;
            mprev[j] = (dc + 16 <= 14 && (dc & 1) == 0) ? 0.f : NEG;
        }
        const f32x4 mfirst = r == 0 ? (f32x4){NEG, NEG, NEG, NEG} : mprev;
        const unsigned pmask = (s & 1) ? 0xffff0000u : 0x0000ffffu;
        const s16x4 ones4 = {0x3f80, 0x3f80, 0x3f80, 0x3f80};
        const float cs = 0.25f * 1.4426950408889634f;
        s16x4 kp[9], vp[9], qp[8];
        #pragma unroll
        for (int i = 0; i < 9; ++i) { kp[i] = pk4(acck[i]); if (i < 8) qp[i] = pk4(accq[i + 1]); }
        f32x4 sp[8], sc[8];
        #pragma unroll
        for (int t = 0; t < 8; ++t) { sp[t] = mfma16(kp[t], qp[t], t == 0 ? mfirst : mprev); sc[t] = mfma16(kp[t + 1], qp[t], mcur); }
        #pragma unroll
        for (int i = 0; i < 9; ++i) vp[i] = pk4(accv[i]);
        SB();
        float a[8][4], mx[8];
        #pragma unroll
        for (int t = 0; t < 8; ++t) {
            a[t][0] = fmx(sp[t][0], sp[t][1]); a[t][1] = fmx(sp[t][2], sp[t][3]);
            a[t][2] = fmx(sc[t][0], sc[t][1]); a[t][3] = fmx(sc[t][2], sc[t][3]);
            mx[t] = fmx(fmx(fmx(a[t][0], a[t][1]), a[t][2]), a[t][3]);
        }
        SB();
        xrow_max4(mx[0], mx[1], mx[2], mx[3]);
        xrow_max4(mx[4], mx[5], mx[6], mx[7]);
        SB();
        s16x4 pp[8], pc[8];
        #pragma unroll
        for (int t = 0; t < 8; ++t) {
            const float nm = -mx[t] * cs;
            float e[4];
            #pragma unroll
            for (int j = 0; j < 4; ++j) e[j] = __builtin_amdgcn_exp2f(__builtin_fmaf(a[t][j], cs, nm));
            pp[t] = __builtin_bit_cast(s16x4, (u32x2){pk2(e[0], e[0]) & pmask, pk2(e[1], e[1]) & pmask});
            pc[t] = __builtin_bit_cast(s16x4, (u32x2){pk2(e[2], e[2]) & pmask, pk2(e[3], e[3]) & pmask});
        }
        SB();
        f32x4 o[8], sm[8];
        #pragma unroll
        for (int t = 0; t < 8; ++t) { sm[t] = mfma16(ones4, pp[t], (f32x4){0, 0, 0, 0}); o[t] = mfma16(vp[t], pp[t], (f32x4){0, 0, 0, 0}); }
        #pragma unroll
        for (int t = 0; t < 8; ++t) { sm[t] = mfma16(ones4, pc[t], sm[t]); o[t] = mfma16(vp[t + 1], pc[t], o[t]); }
        SB();
        float inv[8];
        #pragma unroll
        for (int t = 0; t < 8; ++t) inv[t] = __builtin_amdgcn_rcpf(sm[t][0]);
        #pragma unroll
        for (int t = 0; t < 8; ++t) op[t] = pk4(o[t] * inv[t]);
    }
    {
        const int abase = WS_ACT + (((TB ? 128 : 0) + r * 8) * 16 + (head >> 1)) * 1024 + (head & 1) * 512;
        const int alane = (g & 1) * 16384 + ((g >> 1) * 16 + s) * 16;
        #pragma unroll
        for (int u = 0; u < 4; ++u)
            __builtin_amdgcn_raw_buffer_store_b128(pair16(op[2 * u], op[2 * u + 1]), rws, alane, abase + u * 2 * 16384, 16);
        waitv<0>();
        STAMP(5);
        BAR();
        if (w == 0 && lane == 0) __hip_atomic_store((gu32*)(ws + WS_F2) + (r * 16 + (TB ? 8 : 0) + Q) * 32, tag, RLX_AGENT);
    }
    const int mt = rh2 * 4 + w, r2 = rh2 >> 1;
    unsigned bwo[4];
    auto set_base = [&](int ldsoff) {
        #pragma unroll
        for (int n = 0; n < 4; ++n) { bwo[n] = lds0 + ldsoff + awl + ((n ^ rf) << 5); asm volatile("" : "+v"(bwo[n])); }
    };
    f32x4 oa[4], ob[4];
    #pragma unroll
    for (int n = 0; n < 4; ++n) { oa[n] = (f32x4){0, 0, 0, 0}; ob[n] = (f32x4){0, 0, 0, 0}; }
    s16x4 wl[2][4], wh[2][4];
    auto rdw = [&](int i, int n) { wl[i & 1][n] = trread(bwo[n] + i * 4096); wh[i & 1][n] = trread(bwo[n] + i * 4096 + 512); };
    auto pre = [&](int ldsoff) {
        set_base(ldsoff);
        #pragma unroll
        for (int i = 0; i < 2; ++i)
            #pragma unroll
            for (int n = 0; n < 4; ++n) rdw(i, n);
    };
    auto half = [&](int br, f32x4* acc) {
        u32x4 f[16];
        #pragma unroll
        for (int i = 0; i < 16; ++i) f[i] = __builtin_amdgcn_raw_buffer_load_b128(rws, lane * 16, WS_ACT + ((br * 128 + mt) * 16 + i) * 1024, 16);
        SB();
        #pragma unroll
        for (int i = 0; i < 16; ++i)
            #pragma unroll
            for (int n = 0; n < 4; ++n) {
                acc[n] = mfma32(cat8(wl[i & 1][n], wh[i & 1][n]), __builtin_bit_cast(bf16x8, f[i]), acc[n]);
                if (i + 2 < 16) rdw(i + 2, n);
                SB();
            }
    };
    pre(WO_B);
    wait_flags((const unsigned*)(ws + WS_F2) + (r2 * 16 + 8) * 32, 32, 8, tag, lane);
    STAMP(6);
    half(1, ob);
    STAMP(7);
    pre(WO_A);
    wait_flags((const unsigned*)(ws + WS_F2) + (r2 * 16) * 32, 32, 8, tag, lane);
    STAMP(8);
    const float gt = __uint_as_float(__hip_atomic_load((gu32*)(ws + WS_G) + mt * 16 + s, RLX_AGENT));
    half(0, oa);
    STAMP(9);
    s16x4 res[4];
    #pragma unroll
    for (int n = 0; n < 4; ++n) res[n] = pk4(oa[n] * gt + ob[n] * (1.f - gt));
    char* orow = (char*)out + (size_t)(mt * 16 + s) * 1024 + c2 * 128 + (g & 1) * 32 + (g >> 1) * 16;
    *(u32x4*)orow = pair16(res[0], res[1]);
    *(u32x4*)(orow + 64) = pair16(res[2], res[3]);
    STAMP(10);
}

__global__ __launch_bounds__(512, 2) void fused_kernel(const u16* __restrict__ x, const u16* __restrict__ wqa, const u16* __restrict__ woa,
                                                       const u16* __restrict__ wqb, const u16* __restrict__ wob, const u16* __restrict__ gw,
                                                       u16* __restrict__ out, char* __restrict__ ws) {
    __shared__ __attribute__((aligned(16))) char smem[163840];
    const int lane = threadIdx.x & 63;
    const int wave = __builtin_amdgcn_readfirstlane(threadIdx.x >> 6);
    const int w = wave & 3;
    const int b = blockIdx.x, xcd = b & 7, j = b >> 3;
    const int r = (xcd >> 2) * 8 + (j >> 2);
    const int tb = (j >> 1) & 1, Q = (xcd & 3) * 2 + (j & 1);
    const int rh2 = xcd * 4 + (j >> 3), c2 = j & 7;
    const unsigned lds0 = (unsigned)(size_t)(LAS char*)smem;
    unsigned long long tprobe = 0;
    STAMP(0);
    if (wave >= 4) {
        unsigned tag0 = 0;
        const bool keeper = b == 0 && wave == 4;
        if (keeper) tag0 = __hip_atomic_load((gu32*)(ws + WS_EPOCH), RLX_AGENT) + 1u;
        if (tb) loader<true>(x, wqb, woa, wob, gw, ws, lds0, r, Q, c2, w, lane, wave, tprobe);
        else loader<false>(x, wqa, woa, wob, gw, ws, lds0, r, Q, c2, w, lane, wave, tprobe);
        if (keeper) {
            BAR();
            #pragma unroll 1
            for (int i = 0; i < 4; ++i) wait_flags((const unsigned*)(ws + WS_F2) + i * 64 * 32, 32, 64, tag0, lane);
            if (lane == 0) __hip_atomic_store((gu32*)(ws + WS_EPOCH), tag0, RLX_AGENT);
        }
        return;
    }
    if (tb) matrix<true>(out, ws, lds0, r, Q, rh2, c2, w, lane, wave, tprobe);
    else matrix<false>(out, ws, lds0, r, Q, rh2, c2, w, lane, wave, tprobe);
}

__device__ __forceinline__ void spin40() { unsigned long long t0 = __builtin_amdgcn_s_memrealtime(); while (__builtin_amdgcn_s_memrealtime() - t0 < 4000ull) __builtin_amdgcn_s_sleep(4); }
__global__ void dummy_small(char* ws) { if (ws == nullptr) ws[1] = 0; }
__global__ __launch_bounds__(512, 2) void spin_a(char* ws) { spin40(); if (ws == nullptr) ws[1] = 0; }
__global__ __launch_bounds__(512, 2) void spin_b(char* ws) { asm volatile("v_mov_b32 v205, 0" ::: "v205"); spin40(); if (ws == nullptr) ws[1] = 0; }
__global__ __launch_bounds__(512, 2) void spin_c(char* ws) {
    __shared__ __attribute__((aligned(16))) char smem[163840];
    asm volatile("v_mov_b32 v205, 0" ::: "v205"); spin40();
    if (ws == nullptr) { smem[threadIdx.x] = 1; __syncthreads(); ws[0] = smem[threadIdx.x ^ 1]; }
}
extern "C" void kernel_launch(void* const* d_in, const int* in_sizes, int n_in, void* d_out, int out_size, void* d_ws, size_t ws_size,
                              hipStream_t stream) {
    fused_kernel<<<256, 512, 0, stream>>>((const u16*)d_in[0], (const u16*)d_in[1], (const u16*)d_in[2], (const u16*)d_in[4],
                                          (const u16*)d_in[5], (const u16*)d_in[6], (u16*)d_out, (char*)d_ws);
    spin_c<<<256, 512, 0, stream>>>((char*)d_ws);
}
```

```cpp
#define PROBE 10
#include <hip/hip_runtime.h>
#include <hip/hip_bf16.h>
#include <type_traits>
#define PF_CODE 0
#define PF_TOUCH 0

typedef unsigned short u16;
typedef short s16x4 __attribute__((ext_vector_type(4)));
typedef __bf16 bf16x8 __attribute__((ext_vector_type(8)));
typedef __bf16 bf16x2 __attribute__((ext_vector_type(2)));
typedef float f32x4 __attribute__((ext_vector_type(4)));
typedef float f32x2 __attribute__((ext_vector_type(2)));
typedef unsigned u32x4 __attribute__((ext_vector_type(4)));
typedef unsigned u32x2 __attribute__((ext_vector_type(2)));
#define LAS __attribute__((address_space(3)))
#define GAS __attribute__((address_space(1)))
#define DI __device__ __forceinline__
typedef GAS unsigned gu32;
#define RLX_AGENT __ATOMIC_RELAXED, __HIP_MEMORY_SCOPE_AGENT
template <int V> using IC = std::integral_constant<int, V>;

constexpr int STAGE = 43008;
constexpr int XB = 18432;
constexpr int WO_A = 0;
constexpr int WO_B = 86016;
constexpr int WS_EPOCH = 0;
constexpr int WS_F1 = 4096;
constexpr int WS_F2 = WS_F1 + 512 * 128;
constexpr int WS_G = WS_F2 + 256 * 128;
constexpr int WS_P1 = WS_G + 8192;
constexpr int P1_STRIDE = 1152;
constexpr int WS_ACT = 1 << 20;
constexpr int WS_STAMP = 8 << 20;
constexpr unsigned SPIN_CAP = 1u << 18;

DI void glds(const void* sbase, unsigned voff, unsigned ldsaddr) {
    asm volatile("s_mov_b32 m0, %2\n\ts_nop 0\n\tglobal_load_lds_dwordx4 %0, %1" :: "v"(voff), "s"(sbase), "s"(ldsaddr) : "memory");
}
DI void glds_half(const void* sbase, unsigned voff, unsigned ldsaddr) {
    asm volatile("s_mov_b32 m0, %2\n\ts_mov_b64 exec, 0xffffffff\n\tglobal_load_lds_dwordx4 %0, %1\n\ts_mov_b64 exec, -1" :: "v"(voff), "s"(sbase), "s"(ldsaddr) : "memory");
}
template <int N> DI void waitv() { asm volatile("s_waitcnt vmcnt(%0)" :: "n"(N) : "memory"); }
DI void waitl0() { asm volatile("s_waitcnt lgkmcnt(0)" ::: "memory"); }
#define BAR() __builtin_amdgcn_s_barrier()
#define SB() __builtin_amdgcn_sched_barrier(0)

DI bf16x8 cat8(s16x4 lo, s16x4 hi) { return __builtin_bit_cast(bf16x8, __builtin_shufflevector(lo, hi, 0, 1, 2, 3, 4, 5, 6, 7)); }
DI unsigned pk2(float a, float b) { bf16x2 h = {(__bf16)a, (__bf16)b}; return __builtin_bit_cast(unsigned, h); }
DI s16x4 pk4(f32x4 v) { u32x2 p = {pk2(v[0], v[1]), pk2(v[2], v[3])}; return __builtin_bit_cast(s16x4, p); }
DI f32x4 mfma32(bf16x8 a, bf16x8 b, f32x4 c) { return __builtin_amdgcn_mfma_f32_16x16x32_bf16(a, b, c, 0, 0, 0); }
DI f32x4 mfma16(s16x4 a, s16x4 b, f32x4 c) { return __builtin_amdgcn_mfma_f32_16x16x16bf16_1k(a, b, c, 0, 0, 0); }
DI s16x4 trread(unsigned addr) { return __builtin_amdgcn_ds_read_tr16_b64_v4i16((LAS s16x4*)(size_t)addr); }
DI float fmx(float a, float b) { return __builtin_elementwise_maximum(a, b); }
DI unsigned phi2(float a, float b) {
    f32x2 y = (f32x2){a, b} * 1.4426950408889634f;
    f32x2 e = {__builtin_amdgcn_fmed3f(__builtin_amdgcn_exp2f(y[0]), 0.f, 1.f), __builtin_amdgcn_fmed3f(__builtin_amdgcn_exp2f(y[1]), 0.f, 1.f)};
    f32x2 m = {__int_as_float(max(__float_as_int(a), 0)), __int_as_float(max(__float_as_int(b), 0))};
    f32x2 r = (e + 1e-6f) + m;
    return pk2(r[0], r[1]);
}
DI s16x4 phi4(f32x4 t) { return __builtin_bit_cast(s16x4, (u32x2){phi2(t[0], t[1]), phi2(t[2], t[3])}); }
template <int CTRL> DI float dpp_add(float v) { return v + __builtin_bit_cast(float, __builtin_amdgcn_mov_dpp(__builtin_bit_cast(int, v), CTRL, 0xf, 0xf, true)); }
DI float row_sum16(float v) { v = dpp_add<0x128>(v); v = dpp_add<0x124>(v); v = dpp_add<0x122>(v); return dpp_add<0x121>(v); }
DI void xrow_max4(float& v0, float& v1, float& v2, float& v3) {
    auto a = __builtin_amdgcn_permlane32_swap(__float_as_uint(v0), __float_as_uint(v1), false, false);
    auto b = __builtin_amdgcn_permlane32_swap(__float_as_uint(v2), __float_as_uint(v3), false, false);
    const float c = fmx(__uint_as_float(a[0]), __uint_as_float(a[1]));
    const float d = fmx(__uint_as_float(b[0]), __uint_as_float(b[1]));
    auto e = __builtin_amdgcn_permlane16_swap(__float_as_uint(c), __float_as_uint(d), false, false);
    const unsigned f = __float_as_uint(fmx(__uint_as_float(e[0]), __uint_as_float(e[1])));
    auto h = __builtin_amdgcn_permlane16_swap(f, f, false, false);
    auto p = __builtin_amdgcn_permlane32_swap(h[0], h[0], false, false);
    auto q = __builtin_amdgcn_permlane32_swap(h[1], h[1], false, false);
    v0 = __uint_as_float(p[0]); v1 = __uint_as_float(p[1]); v2 = __uint_as_float(q[0]); v3 = __uint_as_float(q[1]);
}
DI float xrow_sum(float v) {
    auto a = __builtin_amdgcn_permlane32_swap(__float_as_uint(v), __float_as_uint(v), false, false);
    v = __uint_as_float(a[0]) + __uint_as_float(a[1]);
    auto b = __builtin_amdgcn_permlane16_swap(__float_as_uint(v), __float_as_uint(v), false, false);
    return __uint_as_float(b[0]) + __uint_as_float(b[1]);
}
DI u32x4 pair16(s16x4 t0, s16x4 t1) {
    u32x2 a = __builtin_bit_cast(u32x2, t0), b = __builtin_bit_cast(u32x2, t1);
    auto lo = __builtin_amdgcn_permlane16_swap(a[0], b[0], false, false);
    auto hi = __builtin_amdgcn_permlane16_swap(a[1], b[1], false, false);
    return (u32x4){lo[0], hi[0], lo[1], hi[1]};
}
DI void wait_flags(const unsigned* base, int stride, int n, unsigned tag, int lane) {
    const gu32* p = (const gu32*)base + (lane < n ? lane : 0) * stride;
    for (unsigned spins = 0; spins < SPIN_CAP; ++spins) {
        unsigned f = __hip_atomic_load(p, RLX_AGENT);
        if (__all(f == tag)) break;
        __builtin_amdgcn_s_sleep(1);
    }
    __builtin_amdgcn_fence(__ATOMIC_ACQUIRE, "wavefront");
}
#define STAMP(i) do { if ((i) == PROBE) tprobe = __builtin_amdgcn_s_memrealtime(); } while (0)

template <int NM> struct Frags { bf16x8 x[NM]; s16x4 wl[3], wh[3]; };

template <bool TB>
DI void loader(const u16* x, const u16* W, const u16* woa, const u16* wob, const u16* gw, char* ws, unsigned lds0,
               int r, int Q, int c2, int w, int lane, int wave, unsigned long long& tprobe) {
    constexpr int NG = TB ? 11 : 10;
    const int row8 = lane >> 3, pos = lane & 7;
    const int row0 = r * 128;
    const int grow = row0 + ((TB ? 8 : 0) + Q) * 8 + 2 * w;
    u32x4 gx0, gx1, gwv;
    {
        unsigned vo = (unsigned)(grow * 1024 + lane * 16), vg = (unsigned)(lane * 16);
        asm volatile("global_load_dwordx4 %0, %1, %2" : "=v"(gx0) : "v"(vo), "s"(x) : "memory");
        asm volatile("global_load_dwordx4 %0, %1, %2 offset:1024" : "=v"(gx1) : "v"(vo), "s"(x) : "memory");
        asm volatile("global_load_dwordx4 %0, %1, %2" : "=v"(gwv) : "v"(vg), "s"(gw) : "memory");
    }
    const int xsw = ((w & 1) << 2) | (row8 >> 1);
    unsigned vx[4], vxh = 0, vw[6];
    #pragma unroll
    for (int q = 0; q < 4; ++q) vx[q] = (unsigned)((row0 + (w + 4 * q) * 8 + row8) * 1024 + ((pos ^ xsw) << 4));
    if (TB) { int hr = 4 * w + (row8 & 3); int gr = row0 - 16 + hr; if (gr < 0) gr += 16; vxh = (unsigned)(gr * 1024 + ((pos ^ ((hr >> 1) & 7)) << 4)); }
    const int wf = ((row8 >> 1) & 1) | ((w & 1) << 1);
    const int wch = (((pos >> 1) ^ wf) << 1) | (pos & 1);
    #pragma unroll
    for (int i = 0; i < 6; ++i) { int ii = w + 4 * i; vw[i] = (unsigned)(((ii & 7) * 8 + row8) * 3072 + (ii >> 3) * 1024 + Q * 128 + wch * 16); }
    auto issue = [&](int g, int buf, int n) {
        const unsigned lb = lds0 + buf * STAGE;
        if (n < 6) glds((const char*)W + (size_t)g * 64 * 3072, vw[n], lb + XB + (w + 4 * n) * 1024);
        else if (n < 10) glds((const char*)x + g * 128, vx[n - 6], lb + 2048 + (w + 4 * (n - 6)) * 1024);
        else glds_half((const char*)x + g * 128, vxh, lb + w * 512);
    };
    auto issue_group = [&](int g, int buf) {
        #pragma unroll
        for (int n = 0; n < NG; ++n) issue(g, buf, n);
    };
    const unsigned vwo = (unsigned)((w * 8 + row8) * 1024 + c2 * 128 + wch * 16);
    auto issue_wo = [&](const u16* wo, int ldsoff) {
        #pragma unroll
        for (int i = 0; i < 16; ++i) glds((const char*)wo + i * 32768, vwo, lds0 + ldsoff + (w + 4 * i) * 1024);
    };
    issue_group(0, 0);
    #pragma unroll
    for (int n = 0; n < 5; ++n) issue(1, 1, n);
    waitv<5>();
    BAR();
    STAMP(1);
    #pragma unroll
    for (int n = 5; n < NG; ++n) issue(1, 1, n);
    issue_group(2, 2);
    float gsel;
    {
        asm volatile("s_waitcnt vmcnt(%3)" : "+v"(gx0), "+v"(gx1), "+v"(gwv) : "n"(2 * NG) : "memory");
        float d0 = 0.f, d1 = 0.f;
        #pragma unroll
        for (int i = 0; i < 4; ++i) {
            float wl = __uint_as_float(gwv[i] << 16), wh = __uint_as_float(gwv[i] & 0xffff0000u);
            d0 += __uint_as_float(gx0[i] << 16) * wl + __uint_as_float(gx0[i] & 0xffff0000u) * wh;
            d1 += __uint_as_float(gx1[i] << 16) * wl + __uint_as_float(gx1[i] & 0xffff0000u) * wh;
        }
        d0 = xrow_sum(row_sum16(d0)); d1 = xrow_sum(row_sum16(d1));
        float d = lane == 0 ? d0 : d1;
        gsel = __builtin_amdgcn_rcpf(1.f + __builtin_amdgcn_exp2f(-1.4426950408889634f * d));
    }
    int buf = 0;
    #pragma unroll 1
    for (int i = 0; i < 5; ++i) {
        waitv<NG>();
        BAR();
        issue_group(i + 3, buf);
        buf = buf == 2 ? 0 : buf + 1;
    }
    waitv<NG>(); BAR();
    issue_wo(wob, WO_B);
    waitv<16>(); BAR();
    BAR();
    issue_wo(woa, WO_A);
    if (lane < 2) __hip_atomic_store((gu32*)(ws + WS_G) + grow + lane, __float_as_uint(gsel), RLX_AGENT);
    waitv<0>();
    STAMP(2);
}

template <bool TB>
DI void matrix(u16* out, char* ws, unsigned lds0, int r, int Q, int rh2, int c2, int w, int lane, int wave, unsigned long long& tprobe) {
    constexpr int NM = TB ? 9 : 8;
    constexpr int NMF = TB ? 26 : 24;
    constexpr int NRD = NM + 6;
    constexpr int RD0 = 2;
    const unsigned tag = __hip_atomic_load((gu32*)(ws + WS_EPOCH), RLX_AGENT) + 1u;
    const int g = lane >> 4, s = lane & 15;
    const unsigned ax0 = (unsigned)((TB ? 0 : 2048) + s * 128 + ((g ^ (s >> 1)) << 4));
    const int q4 = s >> 2, p4 = lane & 3;
    const int rf = ((q4 >> 1) & 1) | ((g & 1) << 1);
    const unsigned awl = (unsigned)((8 * g + q4) * 128 + p4 * 8);
    f32x4 accq[NM], acck[NM], accv[NM];
    #pragma unroll
    for (int i = 0; i < NM; ++i) { accq[i] = (f32x4){0, 0, 0, 0}; acck[i] = (f32x4){0, 0, 0, 0}; accv[i] = (f32x4){0, 0, 0, 0}; }
    Frags<NM> A, B;
    auto rd_n = [&](Frags<NM>& F, unsigned bwv, unsigned bxv, int n) {
        if (n < 0 || n >= NRD) return;
        if (n < 6) { s16x4 v = trread(bwv + (n >> 1) * 8192 + (n & 1) * 512); if (n & 1) F.wh[n >> 1] = v; else F.wl[n >> 1] = v; }
        else F.x[n - 6] = *(const LAS bf16x8*)(size_t)(bxv + (n - 6) * 2048);
    };
    auto mm = [&](const Frags<NM>& F, int n) {
        int mu, t;
        if (TB) { if (n < 2) { mu = 0; t = n + 1; } else { mu = 1 + (n - 2) / 3; t = (n - 2) % 3; } } else { mu = n / 3; t = n % 3; }
        if (t == 0) accq[mu] = mfma32(cat8(F.wl[0], F.wh[0]), F.x[mu], accq[mu]);
        else if (t == 1) { if (TB) acck[mu] = mfma32(cat8(F.wl[1], F.wh[1]), F.x[mu], acck[mu]); else acck[mu] = mfma32(F.x[mu], cat8(F.wl[1], F.wh[1]), acck[mu]); }
        else accv[mu] = mfma32(F.x[mu], cat8(F.wl[2], F.wh[2]), accv[mu]);
    };
    BAR();
    STAMP(1);
    unsigned bx0 = lds0 + ax0, bx1 = lds0 + (ax0 ^ 64), bw = lds0 + XB + awl + ((w ^ rf) << 5);
    #pragma unroll
    for (int n = 0; n < NRD; ++n) rd_n(A, bw, bx0, n);
    int buf = 0;
    auto stage = [&](auto last_c) {
        constexpr int LAST = decltype(last_c)::value;
        SB();
        #pragma unroll
        for (int n = 0; n < NMF; ++n) { mm(A, n); rd_n(B, bw + 4096, bx1, n - RD0); SB(); }
        const int nbuf = buf == 2 ? 0 : buf + 1;
        const unsigned d = (unsigned)((nbuf - buf) * STAGE);
        bx0 += d; bx1 += d; bw += d; buf = nbuf;
        waitl0();
        BAR();
        SB();
        #pragma unroll
        for (int n = 0; n < NMF; ++n) { mm(B, n); if (!LAST) rd_n(A, bw, bx0, n - RD0); SB(); }
    };
    #pragma unroll 1
    for (int i = 0; i < 7; ++i) stage(IC<0>{});
    stage(IC<1>{});
    STAMP(2);

    const __amdgpu_buffer_rsrc_t rws = __builtin_amdgcn_make_buffer_rsrc(ws, 0, WS_STAMP, 0x00020000);
    const int head = Q * 4 + w;
    s16x4 op[8];
    if (!TB) {
        f32x4 kv = {0, 0, 0, 0}, z = {0, 0, 0, 0};
        const bf16x8 ones = __builtin_bit_cast(bf16x8, (u32x4){0x3f803f80u, 0x3f803f80u, 0x3f803f80u, 0x3f803f80u});
        #pragma unroll
        for (int u = 0; u < 4; ++u) {
            bf16x8 pk = cat8(phi4(acck[2 * u]), phi4(acck[2 * u + 1]));
            bf16x8 vv = cat8(pk4(accv[2 * u]), pk4(accv[2 * u + 1]));
            kv = mfma32(pk, vv, kv);
            z = mfma32(pk, ones, z);
        }
        const int pbase = WS_P1 + (head * 16 + r) * P1_STRIDE;
        __builtin_amdgcn_raw_buffer_store_b128(__builtin_bit_cast(u32x4, kv), rws, lane * 16, pbase, 16);
        if (s == 0) __builtin_amdgcn_raw_buffer_store_b128(__builtin_bit_cast(u32x4, z), rws, 1024 + g * 16, pbase, 16);
        SB();
        s16x4 pq[8];
        #pragma unroll
        for (int t = 0; t < 4; ++t) { pq[t] = phi4(accq[t]); asm volatile("" : "+v"(pq[t])); }
        waitv<0>();
        if (lane == 0) __hip_atomic_store((gu32*)(ws + WS_F1) + (head * 16 + r) * 32, tag, RLX_AGENT);
        STAMP(3);
        #pragma unroll
        for (int t = 4; t < 8; ++t) { pq[t] = phi4(accq[t]); asm volatile("" : "+v"(pq[t])); }
        wait_flags((const unsigned*)(ws + WS_F1) + head * 16 * 32, 32, 16, tag, lane);
        STAMP(4);
        u32x4 part[16];
        const int pb0 = WS_P1 + head * 16 * P1_STRIDE;
        u32x4 zp = __builtin_amdgcn_raw_buffer_load_b128(rws, s * P1_STRIDE + 1024 + g * 16, pb0, 16);
        #pragma unroll
        for (int i = 0; i < 16; ++i) part[i] = __builtin_amdgcn_raw_buffer_load_b128(rws, lane * 16, pb0 + i * P1_STRIDE, 16);
        SB();
        f32x4 zt = __builtin_bit_cast(f32x4, zp);
        #pragma unroll
        for (int i = 0; i < 4; ++i) zt[i] = dpp_add<0x128>(zt[i]);
        #pragma unroll
        for (int i = 0; i < 4; ++i) zt[i] = dpp_add<0x124>(zt[i]);
        #pragma unroll
        for (int i = 0; i < 4; ++i) zt[i] = dpp_add<0x122>(zt[i]);
        #pragma unroll
        for (int i = 0; i < 4; ++i) zt[i] = dpp_add<0x121>(zt[i]);
        const s16x4 za = pk4(zt);
        f32x4 den[8];
        #pragma unroll
        for (int t = 0; t < 8; ++t) den[t] = mfma16(za, pq[t], (f32x4){0, 0, 0, 0});
        SB();
        f32x4 k0 = __builtin_bit_cast(f32x4, part[0]), k1 = __builtin_bit_cast(f32x4, part[1]);
        #pragma unroll
        for (int i = 2; i < 16; i += 2) { k0 += __builtin_bit_cast(f32x4, part[i]); k1 += __builtin_bit_cast(f32x4, part[i + 1]); }
        float inv[8];
        #pragma unroll
        for (int t = 0; t < 8; ++t) inv[t] = __builtin_amdgcn_rcpf(den[t][0] + 1e-6f);
        const s16x4 kva = pk4(k0 + k1);
        SB();
        f32x4 num[8];
        #pragma unroll
        for (int t = 0; t < 8; ++t) num[t] = mfma16(kva, pq[t], (f32x4){0, 0, 0, 0});
        SB();
        #pragma unroll
        for (int t = 0; t < 8; ++t) op[t] = pk4(num[t] * inv[t]);
    } else {
        const float NEG = -1e30f;
        f32x4 mprev, mcur;
        #pragma unroll
        for (int j = 0; j < 4; ++j) {
            const int dc = s - 4 * g - j;
            mcur[j] = (dc >= 0 && dc <= 14 && (dc & 1) == 0) ? 0.f : NEG;
            mprev[j] = (dc + 16 <= 14 && (dc & 1) == 0) ? 0.f : NEG;
        }
        const f32x4 mfirst = r == 0 ? (f32x4){NEG, NEG, NEG, NEG} : mprev;
        const unsigned pmask = (s & 1) ? 0xffff0000u : 0x0000ffffu;
        const s16x4 ones4 = {0x3f80, 0x3f80, 0x3f80, 0x3f80};
        const float cs = 0.25f * 1.4426950408889634f;
        s16x4 kp[9], vp[9], qp[8];
        #pragma unroll
        for (int i = 0; i < 9; ++i) { kp[i] = pk4(acck[i]); if (i < 8) qp[i] = pk4(accq[i + 1]); }
        f32x4 sp[8], sc[8];
        #pragma unroll
        for (int t = 0; t < 8; ++t) { sp[t] = mfma16(kp[t], qp[t], t == 0 ? mfirst : mprev); sc[t] = mfma16(kp[t + 1], qp[t], mcur); }
        #pragma unroll
        for (int i = 0; i < 9; ++i) vp[i] = pk4(accv[i]);
        SB();
        float a[8][4], mx[8];
        #pragma unroll
        for (int t = 0; t < 8; ++t) {
            a[t][0] = fmx(sp[t][0], sp[t][1]); a[t][1] = fmx(sp[t][2], sp[t][3]);
            a[t][2] = fmx(sc[t][0], sc[t][1]); a[t][3] = fmx(sc[t][2], sc[t][3]);
            mx[t] = fmx(fmx(fmx(a[t][0], a[t][1]), a[t][2]), a[t][3]);
        }
        SB();
        xrow_max4(mx[0], mx[1], mx[2], mx[3]);
        xrow_max4(mx[4], mx[5], mx[6], mx[7]);
        SB();
        s16x4 pp[8], pc[8];
        #pragma unroll
        for (int t = 0; t < 8; ++t) {
            const float nm = -mx[t] * cs;
            float e[4];
            #pragma unroll
            for (int j = 0; j < 4; ++j) e[j] = __builtin_amdgcn_exp2f(__builtin_fmaf(a[t][j], cs, nm));
            pp[t] = __builtin_bit_cast(s16x4, (u32x2){pk2(e[0], e[0]) & pmask, pk2(e[1], e[1]) & pmask});
            pc[t] = __builtin_bit_cast(s16x4, (u32x2){pk2(e[2], e[2]) & pmask, pk2(e[3], e[3]) & pmask});
        }
        SB();
        f32x4 o[8], sm[8];
        #pragma unroll
        for (int t = 0; t < 8; ++t) { sm[t] = mfma16(ones4, pp[t], (f32x4){0, 0, 0, 0}); o[t] = mfma16(vp[t], pp[t], (f32x4){0, 0, 0, 0}); }
        #pragma unroll
        for (int t = 0; t < 8; ++t) { sm[t] = mfma16(ones4, pc[t], sm[t]); o[t] = mfma16(vp[t + 1], pc[t], o[t]); }
        SB();
        float inv[8];
        #pragma unroll
        for (int t = 0; t < 8; ++t) inv[t] = __builtin_amdgcn_rcpf(sm[t][0]);
        #pragma unroll
        for (int t = 0; t < 8; ++t) op[t] = pk4(o[t] * inv[t]);
    }
    {
        const int abase = WS_ACT + (((TB ? 128 : 0) + r * 8) * 16 + (head >> 1)) * 1024 + (head & 1) * 512;
        const int alane = (g & 1) * 16384 + ((g >> 1) * 16 + s) * 16;
        #pragma unroll
        for (int u = 0; u < 4; ++u)
            __builtin_amdgcn_raw_buffer_store_b128(pair16(op[2 * u], op[2 * u + 1]), rws, alane, abase + u * 2 * 16384, 16);
        waitv<0>();
        STAMP(5);
        BAR();
        if (w == 0 && lane == 0) __hip_atomic_store((gu32*)(ws + WS_F2) + (r * 16 + (TB ? 8 : 0) + Q) * 32, tag, RLX_AGENT);
    }
    const int mt = rh2 * 4 + w, r2 = rh2 >> 1;
    unsigned bwo[4];
    auto set_base = [&](int ldsoff) {
        #pragma unroll
        for (int n = 0; n < 4; ++n) { bwo[n] = lds0 + ldsoff + awl + ((n ^ rf) << 5); asm volatile("" : "+v"(bwo[n])); }
    };
    f32x4 oa[4], ob[4];
    #pragma unroll
    for (int n = 0; n < 4; ++n) { oa[n] = (f32x4){0, 0, 0, 0}; ob[n] = (f32x4){0, 0, 0, 0}; }
    s16x4 wl[2][4], wh[2][4];
    auto rdw = [&](int i, int n) { wl[i & 1][n] = trread(bwo[n] + i * 4096); wh[i & 1][n] = trread(bwo[n] + i * 4096 + 512); };
    auto pre = [&](int ldsoff) {
        set_base(ldsoff);
        #pragma unroll
        for (int i = 0; i < 2; ++i)
            #pragma unroll
            for (int n = 0; n < 4; ++n) rdw(i, n);
    };
    auto half = [&](int br, f32x4* acc) {
        u32x4 f[16];
        #pragma unroll
        for (int i = 0; i < 16; ++i) f[i] = __builtin_amdgcn_raw_buffer_load_b128(rws, lane * 16, WS_ACT + ((br * 128 + mt) * 16 + i) * 1024, 16);
        SB();
        #pragma unroll
        for (int i = 0; i < 16; ++i)
            #pragma unroll
            for (int n = 0; n < 4; ++n) {
                acc[n] = mfma32(cat8(wl[i & 1][n], wh[i & 1][n]), __builtin_bit_cast(bf16x8, f[i]), acc[n]);
                if (i + 2 < 16) rdw(i + 2, n);
                SB();
            }
    };
    pre(WO_B);
    wait_flags((const unsigned*)(ws + WS_F2) + (r2 * 16 + 8) * 32, 32, 8, tag, lane);
    STAMP(6);
    half(1, ob);
    STAMP(7);
    pre(WO_A);
    wait_flags((const unsigned*)(ws + WS_F2) + (r2 * 16) * 32, 32, 8, tag, lane);
    STAMP(8);
    const float gt = __uint_as_float(__hip_atomic_load((gu32*)(ws + WS_G) + mt * 16 + s, RLX_AGENT));
    half(0, oa);
    STAMP(9);
    s16x4 res[4];
    #pragma unroll
    for (int n = 0; n < 4; ++n) res[n] = pk4(oa[n] * gt + ob[n] * (1.f - gt));
    char* orow = (char*)out + (size_t)(mt * 16 + s) * 1024 + c2 * 128 + (g & 1) * 32 + (g >> 1) * 16;
    *(u32x4*)orow = pair16(res[0], res[1]);
    *(u32x4*)(orow + 64) = pair16(res[2], res[3]);
    STAMP(10);
}

__global__ __launch_bounds__(512, 2) void fused_kernel(const u16* __restrict__ x, const u16* __restrict__ wqa, const u16* __restrict__ woa,
                                                       const u16* __restrict__ wqb, const u16* __restrict__ wob, const u16* __restrict__ gw,
                                                       u16* __restrict__ out, char* __restrict__ ws) {
    __shared__ __attribute__((aligned(16))) char smem[163840];
    const int lane = threadIdx.x & 63;
    const int wave = __builtin_amdgcn_readfirstlane(threadIdx.x >> 6);
    const int w = wave & 3;
    const int b = blockIdx.x, xcd = b & 7, j = b >> 3;
    const int r = (xcd >> 2) * 8 + (j >> 2);
    const int tb = (j >> 1) & 1, Q = (xcd & 3) * 2 + (j & 1);
    const int rh2 = xcd * 4 + (j >> 3), c2 = j & 7;
    const unsigned lds0 = (unsigned)(size_t)(LAS char*)smem;
    unsigned long long tprobe = 0;
    STAMP(0);
    if (wave >= 4) {
        unsigned tag0 = 0;
        const bool keeper = b == 0 && wave == 4;
        if (keeper) tag0 = __hip_atomic_load((gu32*)(ws + WS_EPOCH), RLX_AGENT) + 1u;
        if (tb) loader<true>(x, wqb, woa, wob, gw, ws, lds0, r, Q, c2, w, lane, wave, tprobe);
        else loader<false>(x, wqa, woa, wob, gw, ws, lds0, r, Q, c2, w, lane, wave, tprobe);
        if (keeper) {
            BAR();
            #pragma unroll 1
            for (int i = 0; i < 4; ++i) wait_flags((const unsigned*)(ws + WS_F2) + i * 64 * 32, 32, 64, tag0, lane);
            if (lane == 0) __hip_atomic_store((gu32*)(ws + WS_EPOCH), tag0, RLX_AGENT);
        }
        return;
    }
    if (tb) matrix<true>(out, ws, lds0, r, Q, rh2, c2, w, lane, wave, tprobe);
    else matrix<false>(out, ws, lds0, r, Q, rh2, c2, w, lane, wave, tprobe);
    while (__builtin_amdgcn_s_memrealtime() - tprobe < 4000ull) __builtin_amdgcn_s_sleep(4);
}

__device__ __forceinline__ void spin40() { unsigned long long t0 = __builtin_amdgcn_s_memrealtime(); while (__builtin_amdgcn_s_memrealtime() - t0 < 4000ull) __builtin_amdgcn_s_sleep(4); }
__global__ void dummy_small(char* ws) { if (ws == nullptr) ws[1] = 0; }
__global__ __launch_bounds__(512, 2) void spin_a(char* ws) { spin40(); if (ws == nullptr) ws[1] = 0; }
__global__ __launch_bounds__(512, 2) void spin_b(char* ws) { asm volatile("v_mov_b32 v205, 0" ::: "v205"); spin40(); if (ws == nullptr) ws[1] = 0; }
__global__ __launch_bounds__(512, 2) void spin_c(char* ws) {
    __shared__ __attribute__((aligned(16))) char smem[163840];
    asm volatile("v_mov_b32 v205, 0" ::: "v205"); spin40();
    if (ws == nullptr) { smem[threadIdx.x] = 1; __syncthreads(); ws[0] = smem[threadIdx.x ^ 1]; }
}
extern "C" void kernel_launch(void* const* d_in, const int* in_sizes, int n_in, void* d_out, int out_size, void* d_ws, size_t ws_size,
                              hipStream_t stream) {
    dummy_small<<<1, 64, 0, stream>>>((char*)d_ws);
    fused_kernel<<<256, 512, 0, stream>>>((const u16*)d_in[0], (const u16*)d_in[1], (const u16*)d_in[2], (const u16*)d_in[4],
                                          (const u16*)d_in[5], (const u16*)d_in[6], (u16*)d_out, (char*)d_ws);
}
```

```cpp
#include <hip/hip_runtime.h>
#include <hip/hip_bf16.h>
#include <type_traits>
#define PF_CODE 0
#define PF_TOUCH 0

typedef unsigned short u16;
typedef short s16x4 __attribute__((ext_vector_type(4)));
typedef __bf16 bf16x8 __attribute__((ext_vector_type(8)));
typedef __bf16 bf16x2 __attribute__((ext_vector_type(2)));
typedef float f32x4 __attribute__((ext_vector_type(4)));
typedef float f32x2 __attribute__((ext_vector_type(2)));
typedef unsigned u32x4 __attribute__((ext_vector_type(4)));
typedef unsigned u32x2 __attribute__((ext_vector_type(2)));
#define LAS __attribute__((address_space(3)))
#define GAS __attribute__((address_space(1)))
#define DI __device__ __forceinline__
typedef GAS unsigned gu32;
#define RLX_AGENT __ATOMIC_RELAXED, __HIP_MEMORY_SCOPE_AGENT
template <int V> using IC = std::integral_constant<int, V>;

constexpr int STAGE = 43008;
constexpr int XB = 18432;
constexpr int WO_A = 0;
constexpr int WO_B = 86016;
constexpr int WS_EPOCH = 0;
constexpr int WS_F1 = 4096;
constexpr int WS_F2 = WS_F1 + 512 * 128;
constexpr int WS_G = WS_F2 + 256 * 128;
constexpr int WS_P1 = WS_G + 8192;
constexpr int P1_STRIDE = 1152;
constexpr int WS_ACT = 1 << 20;
constexpr int WS_STAMP = 8 << 20;
constexpr unsigned SPIN_CAP = 1u << 18;

DI void glds(const void* sbase, unsigned voff, unsigned ldsaddr) {
    asm volatile("s_mov_b32 m0, %2\n\ts_nop 0\n\tglobal_load_lds_dwordx4 %0, %1" :: "v"(voff), "s"(sbase), "s"(ldsaddr) : "memory");
}
DI void glds_half(const void* sbase, unsigned voff, unsigned ldsaddr) {
    asm volatile("s_mov_b32 m0, %2\n\ts_mov_b64 exec, 0xffffffff\n\tglobal_load_lds_dwordx4 %0, %1\n\ts_mov_b64 exec, -1" :: "v"(voff), "s"(sbase), "s"(ldsaddr) : "memory");
}
template <int N> DI void waitv() { asm volatile("s_waitcnt vmcnt(%0)" :: "n"(N) : "memory"); }
DI void waitl0() { asm volatile("s_waitcnt lgkmcnt(0)" ::: "memory"); }
#define BAR() __builtin_amdgcn_s_barrier()
#define SB() __builtin_amdgcn_sched_barrier(0)

DI bf16x8 cat8(s16x4 lo, s16x4 hi) { return __builtin_bit_cast(bf16x8, __builtin_shufflevector(lo, hi, 0, 1, 2, 3, 4, 5, 6, 7)); }
DI unsigned pk2(float a, float b) { bf16x2 h = {(__bf16)a, (__bf16)b}; return __builtin_bit_cast(unsigned, h); }
DI s16x4 pk4(f32x4 v) { u32x2 p = {pk2(v[0], v[1]), pk2(v[2], v[3])}; return __builtin_bit_cast(s16x4, p); }
DI f32x4 mfma32(bf16x8 a, bf16x8 b, f32x4 c) { return __builtin_amdgcn_mfma_f32_16x16x32_bf16(a, b, c, 0, 0, 0); }
DI f32x4 mfma16(s16x4 a, s16x4 b, f32x4 c) { return __builtin_amdgcn_mfma_f32_16x16x16bf16_1k(a, b, c, 0, 0, 0); }
DI s16x4 trread(unsigned addr) { return __builtin_amdgcn_ds_read_tr16_b64_v4i16((LAS s16x4*)(size_t)addr); }
DI float fmx(float a, float b) { return __builtin_elementwise_maximum(a, b); }
DI unsigned phi2(float a, float b) {
    f32x2 y = (f32x2){a, b} * 1.4426950408889634f;
    f32x2 e = {__builtin_amdgcn_fmed3f(__builtin_amdgcn_exp2f(y[0]), 0.f, 1.f), __builtin_amdgcn_fmed3f(__builtin_amdgcn_exp2f(y[1]), 0.f, 1.f)};
    f32x2 m = {__int_as_float(max(__float_as_int(a), 0)), __int_as_float(max(__float_as_int(b), 0))};
    f32x2 r = (e + 1e-6f) + m;
    return pk2(r[0], r[1]);
}
DI s16x4 phi4(f32x4 t) { return __builtin_bit_cast(s16x4, (u32x2){phi2(t[0], t[1]), phi2(t[2], t[3])}); }
template <int CTRL> DI float dpp_add(float v) { return v + __builtin_bit_cast(float, __builtin_amdgcn_mov_dpp(__builtin_bit_cast(int, v), CTRL, 0xf, 0xf, true)); }
DI float row_sum16(float v) { v = dpp_add<0x128>(v); v = dpp_add<0x124>(v); v = dpp_add<0x122>(v); return dpp_add<0x121>(v); }
DI void xrow_max4(float& v0, float& v1, float& v2, float& v3) {
    auto a = __builtin_amdgcn_permlane32_swap(__float_as_uint(v0), __float_as_uint(v1), false, false);
    auto b = __builtin_amdgcn_permlane32_swap(__float_as_uint(v2), __float_as_uint(v3), false, false);
    const float c = fmx(__uint_as_float(a[0]), __uint_as_float(a[1]));
    const float d = fmx(__uint_as_float(b[0]), __uint_as_float(b[1]));
    auto e = __builtin_amdgcn_permlane16_swap(__float_as_uint(c), __float_as_uint(d), false, false);
    const unsigned f = __float_as_uint(fmx(__uint_as_float(e[0]), __uint_as_float(e[1])));
    auto h = __builtin_amdgcn_permlane16_swap(f, f, false, false);
    auto p = __builtin_amdgcn_permlane32_swap(h[0], h[0], false, false);
    auto q = __builtin_amdgcn_permlane32_swap(h[1], h[1], false, false);
    v0 = __uint_as_float(p[0]); v1 = __uint_as_float(p[1]); v2 = __uint_as_float(q[0]); v3 = __uint_as_float(q[1]);
}
DI float xrow_sum(float v) {
    auto a = __builtin_amdgcn_permlane32_swap(__float_as_uint(v), __float_as_uint(v), false, false);
    v = __uint_as_float(a[0]) + __uint_as_float(a[1]);
    auto b = __builtin_amdgcn_permlane16_swap(__float_as_uint(v), __float_as_uint(v), false, false);
    return __uint_as_float(b[0]) + __uint_as_float(b[1]);
}
DI u32x4 pair16(s16x4 t0, s16x4 t1) {
    u32x2 a = __builtin_bit_cast(u32x2, t0), b = __builtin_bit_cast(u32x2, t1);
    auto lo = __builtin_amdgcn_permlane16_swap(a[0], b[0], false, false);
    auto hi = __builtin_amdgcn_permlane16_swap(a[1], b[1], false, false);
    return (u32x4){lo[0], hi[0], lo[1], hi[1]};
}
DI void wait_flags(const unsigned* base, int stride, int n, unsigned tag, int lane) {
    const gu32* p = (const gu32*)base + (lane < n ? lane : 0) * stride;
    for (unsigned spins = 0; spins < SPIN_CAP; ++spins) {
        unsigned f = __hip_atomic_load(p, RLX_AGENT);
        if (__all(f == tag)) break;
        __builtin_amdgcn_s_sleep(1);
    }
    __builtin_amdgcn_fence(__ATOMIC_ACQUIRE, "wavefront");
}
#define STAMP(i) do {} while (0)

template <int NM> struct Frags { bf16x8 x[NM]; s16x4 wl[3], wh[3]; };

template <bool TB>
DI void loader(const u16* x, const u16* W, const u16* woa, const u16* wob, const u16* gw, char* ws, unsigned lds0,
               int r, int Q, int c2, int w, int lane, int wave, unsigned long long& tprobe) {
    constexpr int NG = TB ? 11 : 10;
    const int row8 = lane >> 3, pos = lane & 7;
    const int row0 = r * 128;
    const int grow = row0 + ((TB ? 8 : 0) + Q) * 8 + 2 * w;
    u32x4 gx0, gx1, gwv;
    {
        unsigned vo = (unsigned)(grow * 1024 + lane * 16), vg = (unsigned)(lane * 16);
        asm volatile("global_load_dwordx4 %0, %1, %2" : "=v"(gx0) : "v"(vo), "s"(x) : "memory");
        asm volatile("global_load_dwordx4 %0, %1, %2 offset:1024" : "=v"(gx1) : "v"(vo), "s"(x) : "memory");
        asm volatile("global_load_dwordx4 %0, %1, %2" : "=v"(gwv) : "v"(vg), "s"(gw) : "memory");
    }
    const int xsw = ((w & 1) << 2) | (row8 >> 1);
    unsigned vx[4], vxh = 0, vw[6];
    #pragma unroll
    for (int q = 0; q < 4; ++q) vx[q] = (unsigned)((row0 + (w + 4 * q) * 8 + row8) * 1024 + ((pos ^ xsw) << 4));
    if (TB) { int hr = 4 * w + (row8 & 3); int gr = row0 - 16 + hr; if (gr < 0) gr += 16; vxh = (unsigned)(gr * 1024 + ((pos ^ ((hr >> 1) & 7)) << 4)); }
    const int wf = ((row8 >> 1) & 1) | ((w & 1) << 1);
    const int wch = (((pos >> 1) ^ wf) << 1) | (pos & 1);
    #pragma unroll
    for (int i = 0; i < 6; ++i) { int ii = w + 4 * i; vw[i] = (unsigned)(((ii & 7) * 8 + row8) * 3072 + (ii >> 3) * 1024 + Q * 128 + wch * 16); }
    auto issue = [&](int g, int buf, int n) {
        const unsigned lb = lds0 + buf * STAGE;
        if (n < 6) glds((const char*)W + (size_t)g * 64 * 3072, vw[n], lb + XB + (w + 4 * n) * 1024);
        else if (n < 10) glds((const char*)x + g * 128, vx[n - 6], lb + 2048 + (w + 4 * (n - 6)) * 1024);
        else glds_half((const char*)x + g * 128, vxh, lb + w * 512);
    };
    auto issue_group = [&](int g, int buf) {
        #pragma unroll
        for (int n = 0; n < NG; ++n) issue(g, buf, n);
    };
    const unsigned vwo = (unsigned)((w * 8 + row8) * 1024 + c2 * 128 + wch * 16);
    auto issue_wo = [&](const u16* wo, int ldsoff) {
        #pragma unroll
        for (int i = 0; i < 16; ++i) glds((const char*)wo + i * 32768, vwo, lds0 + ldsoff + (w + 4 * i) * 1024);
    };
    issue_group(0, 0);
    #pragma unroll
    for (int n = 0; n < 5; ++n) issue(1, 1, n);
    waitv<5>();
    BAR();
    STAMP(1);
    #pragma unroll
    for (int n = 5; n < NG; ++n) issue(1, 1, n);
    issue_group(2, 2);
    float gsel;
    {
        asm volatile("s_waitcnt vmcnt(%3)" : "+v"(gx0), "+v"(gx1), "+v"(gwv) : "n"(2 * NG) : "memory");
        float d0 = 0.f, d1 = 0.f;
        #pragma unroll
        for (int i = 0; i < 4; ++i) {
            float wl = __uint_as_float(gwv[i] << 16), wh = __uint_as_float(gwv[i] & 0xffff0000u);
            d0 += __uint_as_float(gx0[i] << 16) * wl + __uint_as_float(gx0[i] & 0xffff0000u) * wh;
            d1 += __uint_as_float(gx1[i] << 16) * wl + __uint_as_float(gx1[i] & 0xffff0000u) * wh;
        }
        d0 = xrow_sum(row_sum16(d0)); d1 = xrow_sum(row_sum16(d1));
        float d = lane == 0 ? d0 : d1;
        gsel = __builtin_amdgcn_rcpf(1.f + __builtin_amdgcn_exp2f(-1.4426950408889634f * d));
    }
    int buf = 0;
    #pragma unroll 1
    for (int i = 0; i < 5; ++i) {
        waitv<NG>();
        BAR();
        issue_group(i + 3, buf);
        buf = buf == 2 ? 0 : buf + 1;
    }
    waitv<NG>(); BAR();
    issue_wo(wob, WO_B);
    waitv<16>(); BAR();
    BAR();
    issue_wo(woa, WO_A);
    if (lane < 2) __hip_atomic_store((gu32*)(ws + WS_G) + grow + lane, __float_as_uint(gsel), RLX_AGENT);
    waitv<0>();
    STAMP(2);
}

template <bool TB>
DI void matrix(u16* out, char* ws, unsigned lds0, int r, int Q, int rh2, int c2, int w, int lane, int wave, unsigned long long& tprobe) {
    constexpr int NM = TB ? 9 : 8;
    constexpr int NMF = TB ? 26 : 24;
    constexpr int NRD = NM + 6;
    constexpr int RD0 = 2;
    const unsigned tag = __hip_atomic_load((gu32*)(ws + WS_EPOCH), RLX_AGENT) + 1u;
    const int g = lane >> 4, s = lane & 15;
    const unsigned ax0 = (unsigned)((TB ? 0 : 2048) + s * 128 + ((g ^ (s >> 1)) << 4));
    const int q4 = s >> 2, p4 = lane & 3;
    const int rf = ((q4 >> 1) & 1) | ((g & 1) << 1);
    const unsigned awl = (unsigned)((8 * g + q4) * 128 + p4 * 8);
    f32x4 accq[NM], acck[NM], accv[NM];
    #pragma unroll
    for (int i = 0; i < NM; ++i) { accq[i] = (f32x4){0, 0, 0, 0}; acck[i] = (f32x4){0, 0, 0, 0}; accv[i] = (f32x4){0, 0, 0, 0}; }
    Frags<NM> A, B;
    auto rd_n = [&](Frags<NM>& F, unsigned bwv, unsigned bxv, int n) {
        if (n < 0 || n >= NRD) return;
        if (n < 6) { s16x4 v = trread(bwv + (n >> 1) * 8192 + (n & 1) * 512); if (n & 1) F.wh[n >> 1] = v; else F.wl[n >> 1] = v; }
        else F.x[n - 6] = *(const LAS bf16x8*)(size_t)(bxv + (n - 6) * 2048);
    };
    auto mm = [&](const Frags<NM>& F, int n) {
        int mu, t;
        if (TB) { if (n < 2) { mu = 0; t = n + 1; } else { mu = 1 + (n - 2) / 3; t = (n - 2) % 3; } } else { mu = n / 3; t = n % 3; }
        if (t == 0) accq[mu] = mfma32(cat8(F.wl[0], F.wh[0]), F.x[mu], accq[mu]);
        else if (t == 1) { if (TB) acck[mu] = mfma32(cat8(F.wl[1], F.wh[1]), F.x[mu], acck[mu]); else acck[mu] = mfma32(F.x[mu], cat8(F.wl[1], F.wh[1]), acck[mu]); }
        else accv[mu] = mfma32(F.x[mu], cat8(F.wl[2], F.wh[2]), accv[mu]);
    };
    BAR();
    STAMP(1);
    unsigned bx0 = lds0 + ax0, bx1 = lds0 + (ax0 ^ 64), bw = lds0 + XB + awl + ((w ^ rf) << 5);
    #pragma unroll
    for (int n = 0; n < NRD; ++n) rd_n(A, bw, bx0, n);
    int buf = 0;
    auto stage = [&](auto last_c) {
        constexpr int LAST = decltype(last_c)::value;
        SB();
        #pragma unroll
        for (int n = 0; n < NMF; ++n) { mm(A, n); rd_n(B, bw + 4096, bx1, n - RD0); SB(); }
        const int nbuf = buf == 2 ? 0 : buf + 1;
        const unsigned d = (unsigned)((nbuf - buf) * STAGE);
        bx0 += d; bx1 += d; bw += d; buf = nbuf;
        waitl0();
        BAR();
        SB();
        #pragma unroll
        for (int n = 0; n < NMF; ++n) { mm(B, n); if (!LAST) rd_n(A, bw, bx0, n - RD0); SB(); }
    };
    #pragma unroll 1
    for (int i = 0; i < 7; ++i) stage(IC<0>{});
    stage(IC<1>{});
    STAMP(2);

    const __amdgpu_buffer_rsrc_t rws = __builtin_amdgcn_make_buffer_rsrc(ws, 0, WS_STAMP, 0x00020000);
    const int head = Q * 4 + w;
    s16x4 op[8];
    if (!TB) {
        f32x4 kv = {0, 0, 0, 0}, z = {0, 0, 0, 0};
        const bf16x8 ones = __builtin_bit_cast(bf16x8, (u32x4){0x3f803f80u, 0x3f803f80u, 0x3f803f80u, 0x3f803f80u});
        #pragma unroll
        for (int u = 0; u < 4; ++u) {
            bf16x8 pk = cat8(phi4(acck[2 * u]), phi4(acck[2 * u + 1]));
            bf16x8 vv = cat8(pk4(accv[2 * u]), pk4(accv[2 * u + 1]));
            kv = mfma32(pk, vv, kv);
            z = mfma32(pk, ones, z);
        }
        const int pbase = WS_P1 + (head * 16 + r) * P1_STRIDE;
        __builtin_amdgcn_raw_buffer_store_b128(__builtin_bit_cast(u32x4, kv), rws, lane * 16, pbase, 16);
        if (s == 0) __builtin_amdgcn_raw_buffer_store_b128(__builtin_bit_cast(u32x4, z), rws, 1024 + g * 16, pbase, 16);
        SB();
        s16x4 pq[8];
        #pragma unroll
        for (int t = 0; t < 4; ++t) { pq[t] = phi4(accq[t]); asm volatile("" : "+v"(pq[t])); }
        waitv<0>();
        if (lane == 0) __hip_atomic_store((gu32*)(ws + WS_F1) + (head * 16 + r) * 32, tag, RLX_AGENT);
        STAMP(3);
        #pragma unroll
        for (int t = 4; t < 8; ++t) { pq[t] = phi4(accq[t]); asm volatile("" : "+v"(pq[t])); }
        wait_flags((const unsigned*)(ws + WS_F1) + head * 16 * 32, 32, 16, tag, lane);
        STAMP(4);
        u32x4 part[16];
        const int pb0 = WS_P1 + head * 16 * P1_STRIDE;
        u32x4 zp = __builtin_amdgcn_raw_buffer_load_b128(rws, s * P1_STRIDE + 1024 + g * 16, pb0, 16);
        #pragma unroll
        for (int i = 0; i < 16; ++i) part[i] = __builtin_amdgcn_raw_buffer_load_b128(rws, lane * 16, pb0 + i * P1_STRIDE, 16);
        SB();
        f32x4 zt = __builtin_bit_cast(f32x4, zp);
        #pragma unroll
        for (int i = 0; i < 4; ++i) zt[i] = dpp_add<0x128>(zt[i]);
        #pragma unroll
        for (int i = 0; i < 4; ++i) zt[i] = dpp_add<0x124>(zt[i]);
        #pragma unroll
        for (int i = 0; i < 4; ++i) zt[i] = dpp_add<0x122>(zt[i]);
        #pragma unroll
        for (int i = 0; i < 4; ++i) zt[i] = dpp_add<0x121>(zt[i]);
        const s16x4 za = pk4(zt);
        f32x4 den[8];
        #pragma unroll
        for (int t = 0; t < 8; ++t) den[t] = mfma16(za, pq[t], (f32x4){0, 0, 0, 0});
        SB();
        f32x4 k0 = __builtin_bit_cast(f32x4, part[0]), k1 = __builtin_bit_cast(f32x4, part[1]);
        #pragma unroll
        for (int i = 2; i < 16; i += 2) { k0 += __builtin_bit_cast(f32x4, part[i]); k1 += __builtin_bit_cast(f32x4, part[i + 1]); }
        float inv[8];
        #pragma unroll
        for (int t = 0; t < 8; ++t) inv[t] = __builtin_amdgcn_rcpf(den[t][0] + 1e-6f);
        const s16x4 kva = pk4(k0 + k1);
        SB();
        f32x4 num[8];
        #pragma unroll
        for (int t = 0; t < 8; ++t) num[t] = mfma16(kva, pq[t], (f32x4){0, 0, 0, 0});
        SB();
        #pragma unroll
        for (int t = 0; t < 8; ++t) op[t] = pk4(num[t] * inv[t]);
    } else {
        const float NEG = -1e30f;
        f32x4 mprev, mcur;
        #pragma unroll
        for (int j = 0; j < 4; ++j) {
            const int dc = s - 4 * g - j;
            mcur[j] = (dc >= 0 && dc <= 14 && (dc & 1) == 0) ? 0.f : NEG;
            mprev[j] = (dc + 16 <= 14 && (dc & 1) == 0) ? 0.f : NEG;
        }
        const f32x4 mfirst = r == 0 ? (f32x4){NEG, NEG, NEG, NEG} : mprev;
        const unsigned pmask = (s & 1) ? 0xffff0000u : 0x0000ffffu;
        const s16x4 ones4 = {0x3f80, 0x3f80, 0x3f80, 0x3f80};
        const float cs = 0.25f * 1.4426950408889634f;
        s16x4 kp[9], vp[9], qp[8];
        #pragma unroll
        for (int i = 0; i < 9; ++i) { kp[i] = pk4(acck[i]); if (i < 8) qp[i] = pk4(accq[i + 1]); }
        f32x4 sp[8], sc[8];
        #pragma unroll
        for (int t = 0; t < 8; ++t) { sp[t] = mfma16(kp[t], qp[t], t == 0 ? mfirst : mprev); sc[t] = mfma16(kp[t + 1], qp[t], mcur); }
        #pragma unroll
        for (int i = 0; i < 9; ++i) vp[i] = pk4(accv[i]);
        SB();
        float a[8][4], mx[8];
        #pragma unroll
        for (int t = 0; t < 8; ++t) {
            a[t][0] = fmx(sp[t][0], sp[t][1]); a[t][1] = fmx(sp[t][2], sp[t][3]);
            a[t][2] = fmx(sc[t][0], sc[t][1]); a[t][3] = fmx(sc[t][2], sc[t][3]);
            mx[t] = fmx(fmx(fmx(a[t][0], a[t][1]), a[t][2]), a[t][3]);
        }
        SB();
        xrow_max4(mx[0], mx[1], mx[2], mx[3]);
        xrow_max4(mx[4], mx[5], mx[6], mx[7]);
        SB();
        s16x4 pp[8], pc[8];
        #pragma unroll
        for (int t = 0; t < 8; ++t) {
            const float nm = -mx[t] * cs;
            float e[4];
            #pragma unroll
            for (int j = 0; j < 4; ++j) e[j] = __builtin_amdgcn_exp2f(__builtin_fmaf(a[t][j], cs, nm));
            pp[t] = __builtin_bit_cast(s16x4, (u32x2){pk2(e[0], e[0]) & pmask, pk2(e[1], e[1]) & pmask});
            pc[t] = __builtin_bit_cast(s16x4, (u32x2){pk2(e[2], e[2]) & pmask, pk2(e[3], e[3]) & pmask});
        }
        SB();
        f32x4 o[8], sm[8];
        #pragma unroll
        for (int t = 0; t < 8; ++t) { sm[t] = mfma16(ones4, pp[t], (f32x4){0, 0, 0, 0}); o[t] = mfma16(vp[t], pp[t], (f32x4){0, 0, 0, 0}); }
        #pragma unroll
        for (int t = 0; t < 8; ++t) { sm[t] = mfma16(ones4, pc[t], sm[t]); o[t] = mfma16(vp[t + 1], pc[t], o[t]); }
        SB();
        float inv[8];
        #pragma unroll
        for (int t = 0; t < 8; ++t) inv[t] = __builtin_amdgcn_rcpf(sm[t][0]);
        #pragma unroll
        for (int t = 0; t < 8; ++t) op[t] = pk4(o[t] * inv[t]);
    }
    {
        const int abase = WS_ACT + (((TB ? 128 : 0) + r * 8) * 16 + (head >> 1)) * 1024 + (head & 1) * 512;
        const int alane = (g & 1) * 16384 + ((g >> 1) * 16 + s) * 16;
        #pragma unroll
        for (int u = 0; u < 4; ++u)
            __builtin_amdgcn_raw_buffer_store_b128(pair16(op[2 * u], op[2 * u + 1]), rws, alane, abase + u * 2 * 16384, 16);
        waitv<0>();
        STAMP(5);
        BAR();
        if (w == 0 && lane == 0) __hip_atomic_store((gu32*)(ws + WS_F2) + (r * 16 + (TB ? 8 : 0) + Q) * 32, tag, RLX_AGENT);
    }
    const int mt = rh2 * 4 + w, r2 = rh2 >> 1;
    unsigned bwo[4];
    auto set_base = [&](int ldsoff) {
        #pragma unroll
        for (int n = 0; n < 4; ++n) { bwo[n] = lds0 + ldsoff + awl + ((n ^ rf) << 5); asm volatile("" : "+v"(bwo[n])); }
    };
    f32x4 oa[4], ob[4];
    #pragma unroll
    for (int n = 0; n < 4; ++n) { oa[n] = (f32x4){0, 0, 0, 0}; ob[n] = (f32x4){0, 0, 0, 0}; }
    s16x4 wl[2][4], wh[2][4];
    auto rdw = [&](int i, int n) { wl[i & 1][n] = trread(bwo[n] + i * 4096); wh[i & 1][n] = trread(bwo[n] + i * 4096 + 512); };
    auto pre = [&](int ldsoff) {
        set_base(ldsoff);
        #pragma unroll
        for (int i = 0; i < 2; ++i)
            #pragma unroll
            for (int n = 0; n < 4; ++n) rdw(i, n);
    };
    auto half = [&](int br, f32x4* acc) {
        u32x4 f[16];
        #pragma unroll
        for (int i = 0; i < 16; ++i) f[i] = __builtin_amdgcn_raw_buffer_load_b128(rws, lane * 16, WS_ACT + ((br * 128 + mt) * 16 + i) * 1024, 16);
        SB();
        #pragma unroll
        for (int i = 0; i < 16; ++i)
            #pragma unroll
            for (int n = 0; n < 4; ++n) {
                acc[n] = mfma32(cat8(wl[i & 1][n], wh[i & 1][n]), __builtin_bit_cast(bf16x8, f[i]), acc[n]);
                if (i + 2 < 16) rdw(i + 2, n);
                SB();
            }
    };
    pre(WO_B);
    wait_flags((const unsigned*)(ws + WS_F2) + (r2 * 16 + 8) * 32, 32, 8, tag, lane);
    STAMP(6);
    half(1, ob);
    STAMP(7);
    pre(WO_A);
    wait_flags((const unsigned*)(ws + WS_F2) + (r2 * 16) * 32, 32, 8, tag, lane);
    STAMP(8);
    const float gt = __uint_as_float(__hip_atomic_load((gu32*)(ws + WS_G) + mt * 16 + s, RLX_AGENT));
    half(0, oa);
    STAMP(9);
    s16x4 res[4];
    #pragma unroll
    for (int n = 0; n < 4; ++n) res[n] = pk4(oa[n] * gt + ob[n] * (1.f - gt));
    char* orow = (char*)out + (size_t)(mt * 16 + s) * 1024 + c2 * 128 + (g & 1) * 32 + (g >> 1) * 16;
    *(u32x4*)orow = pair16(res[0], res[1]);
    *(u32x4*)(orow + 64) = pair16(res[2], res[3]);
    STAMP(10);
}

__global__ __launch_bounds__(512, 2) void fused_kernel(const u16* __restrict__ x, const u16* __restrict__ wqa, const u16* __restrict__ woa,
                                                       const u16* __restrict__ wqb, const u16* __restrict__ wob, const u16* __restrict__ gw,
                                                       u16* __restrict__ out, char* __restrict__ ws) {
    __shared__ __attribute__((aligned(16))) char smem[163840];
    const int lane = threadIdx.x & 63;
    const int wave = __builtin_amdgcn_readfirstlane(threadIdx.x >> 6);
    const int w = wave & 3;
    const int b = blockIdx.x, xcd = b & 7, j = b >> 3;
    const int r = (xcd >> 2) * 8 + (j >> 2);
    const int tb = (j >> 1) & 1, Q = (xcd & 3) * 2 + (j & 1);
    const int rh2 = xcd * 4 + (j >> 3), c2 = j & 7;
    const unsigned lds0 = (unsigned)(size_t)(LAS char*)smem;
    unsigned long long tprobe = 0;
    STAMP(0);
    if (wave >= 4) {
        unsigned tag0 = 0;
        const bool keeper = b == 0 && wave == 4;
        if (keeper) tag0 = __hip_atomic_load((gu32*)(ws + WS_EPOCH), RLX_AGENT) + 1u;
        if (tb) loader<true>(x, wqb, woa, wob, gw, ws, lds0, r, Q, c2, w, lane, wave, tprobe);
        else loader<false>(x, wqa, woa, wob, gw, ws, lds0, r, Q, c2, w, lane, wave, tprobe);
        if (keeper) {
            BAR();
            #pragma unroll 1
            for (int i = 0; i < 4; ++i) wait_flags((const unsigned*)(ws + WS_F2) + i * 64 * 32, 32, 64, tag0, lane);
            if (lane == 0) __hip_atomic_store((gu32*)(ws + WS_EPOCH), tag0, RLX_AGENT);
        }
        return;
    }
    if (tb) matrix<true>(out, ws, lds0, r, Q, rh2, c2, w, lane, wave, tprobe);
    else matrix<false>(out, ws, lds0, r, Q, rh2, c2, w, lane, wave, tprobe);
}

__device__ __forceinline__ void spin40() { unsigned long long t0 = __builtin_amdgcn_s_memrealtime(); while (__builtin_amdgcn_s_memrealtime() - t0 < 4000ull) __builtin_amdgcn_s_sleep(4); }
__global__ void dummy_small(char* ws) { if (ws == nullptr) ws[1] = 0; }
__global__ __launch_bounds__(512, 2) void spin_a(char* ws) { spin40(); if (ws == nullptr) ws[1] = 0; }
__global__ __launch_bounds__(512, 2) void spin_b(char* ws) { asm volatile("v_mov_b32 v205, 0" ::: "v205"); spin40(); if (ws == nullptr) ws[1] = 0; }
__global__ __launch_bounds__(512, 2) void spin_c(char* ws) {
    __shared__ __attribute__((aligned(16))) char smem[163840];
    asm volatile("v_mov_b32 v205, 0" ::: "v205"); spin40();
    if (ws == nullptr) { smem[threadIdx.x] = 1; __syncthreads(); ws[0] = smem[threadIdx.x ^ 1]; }
}
extern "C" void kernel_launch(void* const* d_in, const int* in_sizes, int n_in, void* d_out, int out_size, void* d_ws, size_t ws_size,
                              hipStream_t stream) {
    fused_kernel<<<256, 512, 0, stream>>>((const u16*)d_in[0], (const u16*)d_in[1], (const u16*)d_in[2], (const u16*)d_in[4],
                                          (const u16*)d_in[5], (const u16*)d_in[6], (u16*)d_out, (char*)d_ws);
    spin_a<<<256, 512, 0, stream>>>((char*)d_ws);
}
```

```cpp
#include <hip/hip_runtime.h>
#include <hip/hip_bf16.h>
#include <type_traits>

typedef unsigned short u16;
typedef short s16x4 __attribute__((ext_vector_type(4)));
typedef __bf16 bf16x8 __attribute__((ext_vector_type(8)));
typedef __bf16 bf16x2 __attribute__((ext_vector_type(2)));
typedef float f32x4 __attribute__((ext_vector_type(4)));
typedef float f32x2 __attribute__((ext_vector_type(2)));
typedef unsigned u32x4 __attribute__((ext_vector_type(4)));
typedef unsigned u32x2 __attribute__((ext_vector_type(2)));
#define LAS __attribute__((address_space(3)))
#define GAS __attribute__((address_space(1)))
#define DI __device__ __forceinline__
typedef GAS unsigned gu32;
typedef LAS unsigned lu32;
#define RLX_AGENT __ATOMIC_RELAXED, __HIP_MEMORY_SCOPE_AGENT
template <int V> using IC = std::integral_constant<int, V>;

constexpr int STAGE = 43008;
constexpr int XB = 18432;
constexpr int WO_A = 0;
constexpr int WO_B = 86016;
constexpr int LDS_RESB = 66560;
constexpr int LDS_CNT = 151552;
constexpr int LDS_GATE = 151808;
constexpr int LDS_JUNK = 152064;
constexpr int CNT_WO = 0, CNT_PUB = 1, CNT_BF = 2, CNT_AF = 3, CNT_RES = 4, CNT_PS = 8;
constexpr int WS_EPOCH = 0;
constexpr int WS_F1 = 4096;
constexpr int WS_F2 = WS_F1 + 512 * 128;
constexpr int WS_G = WS_F2 + 256 * 128;
constexpr int WS_P1 = WS_G + 256 * 128;
constexpr int P1_STRIDE = 640;
constexpr int WS_ACT = 1 << 20;
constexpr int WS_END = 8 << 20;
constexpr unsigned SPIN_CAP = 1u << 18;
static_assert(WS_P1 + 512 * P1_STRIDE <= WS_ACT && WS_ACT + 256 * 16384 <= WS_END, "workspace map");

DI void glds(const void* sbase, unsigned voff, unsigned ldsaddr) {
    asm volatile("s_mov_b32 m0, %2\n\ts_nop 0\n\tglobal_load_lds_dwordx4 %0, %1" :: "v"(voff), "s"(sbase), "s"(ldsaddr) : "memory");
}
DI void glds_half(const void* sbase, unsigned voff, unsigned ldsaddr) {
    asm volatile("s_mov_b32 m0, %2\n\ts_mov_b64 exec, 0xffffffff\n\tglobal_load_lds_dwordx4 %0, %1\n\ts_mov_b64 exec, -1" :: "v"(voff), "s"(sbase), "s"(ldsaddr) : "memory");
}
DI void glds4s(const void* sbase, unsigned voff, unsigned ldsaddr) {
    asm volatile("s_mov_b32 m0, %2\n\ts_nop 0\n\tglobal_load_lds_dword %0, %1" :: "v"(voff), "s"(sbase), "s"(ldsaddr) : "memory");
}
DI void glds4(const void* p, unsigned ldsaddr) {
    asm volatile("s_mov_b32 m0, %1\n\ts_nop 0\n\tglobal_load_lds_dword %0, off" :: "v"(p), "s"(ldsaddr) : "memory");
}
template <int N> DI void waitv() { asm volatile("s_waitcnt vmcnt(%0)" :: "n"(N) : "memory"); }
DI void waitl0() { asm volatile("s_waitcnt lgkmcnt(0)" ::: "memory"); }
#define BAR() __builtin_amdgcn_s_barrier()
#define SB() __builtin_amdgcn_sched_barrier(0)

DI bf16x8 cat8(s16x4 lo, s16x4 hi) { return __builtin_bit_cast(bf16x8, __builtin_shufflevector(lo, hi, 0, 1, 2, 3, 4, 5, 6, 7)); }
DI unsigned pk2(float a, float b) { bf16x2 h = {(__bf16)a, (__bf16)b}; return __builtin_bit_cast(unsigned, h); }
DI s16x4 pk4(f32x4 v) { u32x2 p = {pk2(v[0], v[1]), pk2(v[2], v[3])}; return __builtin_bit_cast(s16x4, p); }
DI f32x4 mfma32(bf16x8 a, bf16x8 b, f32x4 c) { return __builtin_amdgcn_mfma_f32_16x16x32_bf16(a, b, c, 0, 0, 0); }
DI f32x4 mfma16(s16x4 a, s16x4 b, f32x4 c) { return __builtin_amdgcn_mfma_f32_16x16x16bf16_1k(a, b, c, 0, 0, 0); }
DI s16x4 trread(unsigned addr) { return __builtin_amdgcn_ds_read_tr16_b64_v4i16((LAS s16x4*)(size_t)addr); }
DI float fmx(float a, float b) { return __builtin_elementwise_maximum(a, b); }
DI unsigned phi2(float a, float b) {
    f32x2 y = (f32x2){a, b} * 1.4426950408889634f;
    f32x2 e = {__builtin_amdgcn_fmed3f(__builtin_amdgcn_exp2f(y[0]), 0.f, 1.f), __builtin_amdgcn_fmed3f(__builtin_amdgcn_exp2f(y[1]), 0.f, 1.f)};
    f32x2 m = {__int_as_float(max(__float_as_int(a), 0)), __int_as_float(max(__float_as_int(b), 0))};
    f32x2 r = (e + 1e-6f) + m;
    return pk2(r[0], r[1]);
}
DI s16x4 phi4(f32x4 t) { return __builtin_bit_cast(s16x4, (u32x2){phi2(t[0], t[1]), phi2(t[2], t[3])}); }
template <int CTRL> DI float dpp_add(float v) { return v + __builtin_bit_cast(float, __builtin_amdgcn_mov_dpp(__builtin_bit_cast(int, v), CTRL, 0xf, 0xf, true)); }
DI float row_sum16(float v) { v = dpp_add<0x128>(v); v = dpp_add<0x124>(v); v = dpp_add<0x122>(v); return dpp_add<0x121>(v); }
DI void row_sum16x4(f32x4& v) {
    float a = v[0], b = v[1], c = v[2], d = v[3];
#define DPP4(ctl) "v_add_f32_dpp %0, %0, %0 " ctl " row_mask:0xf bank_mask:0xf\n\tv_add_f32_dpp %1, %1, %1 " ctl " row_mask:0xf bank_mask:0xf\n\t" \
                  "v_add_f32_dpp %2, %2, %2 " ctl " row_mask:0xf bank_mask:0xf\n\tv_add_f32_dpp %3, %3, %3 " ctl " row_mask:0xf bank_mask:0xf\n\t"
    asm("s_nop 1\n\t" DPP4("row_ror:8") DPP4("row_ror:4") DPP4("row_ror:2") DPP4("row_ror:1") "s_nop 0" : "+v"(a), "+v"(b), "+v"(c), "+v"(d));
#undef DPP4
    v = (f32x4){a, b, c, d};
}
DI float xrow_sum(float v) {
    auto a = __builtin_amdgcn_permlane32_swap(__float_as_uint(v), __float_as_uint(v), false, false);
    v = __uint_as_float(a[0]) + __uint_as_float(a[1]);
    auto b = __builtin_amdgcn_permlane16_swap(__float_as_uint(v), __float_as_uint(v), false, false);
    return __uint_as_float(b[0]) + __uint_as_float(b[1]);
}
DI void xrow_max4(float& v0, float& v1, float& v2, float& v3) {
    auto a = __builtin_amdgcn_permlane32_swap(__float_as_uint(v0), __float_as_uint(v1), false, false);
    auto b = __builtin_amdgcn_permlane32_swap(__float_as_uint(v2), __float_as_uint(v3), false, false);
    const float c = fmx(__uint_as_float(a[0]), __uint_as_float(a[1]));
    const float d = fmx(__uint_as_float(b[0]), __uint_as_float(b[1]));
    auto e = __builtin_amdgcn_permlane16_swap(__float_as_uint(c), __float_as_uint(d), false, false);
    const unsigned f = __float_as_uint(fmx(__uint_as_float(e[0]), __uint_as_float(e[1])));
    auto h = __builtin_amdgcn_permlane16_swap(f, f, false, false);
    auto p = __builtin_amdgcn_permlane32_swap(h[0], h[0], false, false);
    auto q = __builtin_amdgcn_permlane32_swap(h[1], h[1], false, false);
    v0 = __uint_as_float(p[0]); v1 = __uint_as_float(p[1]); v2 = __uint_as_float(q[0]); v3 = __uint_as_float(q[1]);
}
DI u32x4 pair16(s16x4 t0, s16x4 t1) {
    u32x2 a = __builtin_bit_cast(u32x2, t0), b = __builtin_bit_cast(u32x2, t1);
    auto lo = __builtin_amdgcn_permlane16_swap(a[0], b[0], false, false);
    auto hi = __builtin_amdgcn_permlane16_swap(a[1], b[1], false, false);
    return (u32x4){lo[0], hi[0], lo[1], hi[1]};
}

template <int SL> DI void wait_flags(const char* p, unsigned tag) {
    for (unsigned spins = 0; spins < SPIN_CAP; ++spins) {
        if (__all(__hip_atomic_load((const gu32*)p, RLX_AGENT) == tag)) break;
        __builtin_amdgcn_s_sleep(SL);
    }
    __builtin_amdgcn_fence(__ATOMIC_ACQUIRE, "wavefront");
}
DI void flag_set(char* p, unsigned tag) { __hip_atomic_store((gu32*)p, tag, RLX_AGENT); }
DI unsigned lds_inc(unsigned addr) { return __hip_atomic_fetch_add((lu32*)(size_t)addr, 1u, __ATOMIC_RELAXED, __HIP_MEMORY_SCOPE_WORKGROUP); }
template <int SL> DI void lds_wait(unsigned addr, unsigned want) {
    for (unsigned spins = 0; spins < SPIN_CAP; ++spins) {
        if (__hip_atomic_load((lu32*)(size_t)addr, __ATOMIC_RELAXED, __HIP_MEMORY_SCOPE_WORKGROUP) == want) break;
        __builtin_amdgcn_s_sleep(SL);
    }
    asm volatile("" ::: "memory");
}

struct OutHalf {
    unsigned bwo[4];
    s16x4 wl[2][4], wh[2][4];
    DI void rdw(int i, int n) { wl[i & 1][n] = trread(bwo[n] + i * 4096); wh[i & 1][n] = trread(bwo[n] + i * 4096 + 512); }
    DI void pre(unsigned base, int rf) {
        #pragma unroll
        for (int n = 0; n < 4; ++n) { bwo[n] = base + ((n ^ rf) << 5); asm volatile("" : "+v"(bwo[n])); }
        #pragma unroll
        for (int i = 0; i < 2; ++i)
            #pragma unroll
            for (int n = 0; n < 4; ++n) rdw(i, n);
    }
    DI void run(__amdgpu_buffer_rsrc_t rws, int abase, int lane, f32x4* acc) {
        u32x4 f[16];
        #pragma unroll
        for (int i = 0; i < 16; ++i) f[i] = __builtin_amdgcn_raw_buffer_load_b128(rws, lane * 16, abase + i * 1024, 16);
        SB();
        #pragma unroll
        for (int i = 0; i < 16; ++i)
            #pragma unroll
            for (int n = 0; n < 4; ++n) {
                acc[n] = mfma32(cat8(wl[i & 1][n], wh[i & 1][n]), __builtin_bit_cast(bf16x8, f[i]), acc[n]);
                if (i + 2 < 16) rdw(i + 2, n);
                SB();
            }
    }
};

struct LoaderOffs { unsigned vx[4], vw[6], vxh; int wch; };
DI LoaderOffs loader_offsets(int r, int Q, int w, int lane) {
    LoaderOffs lo;
    const int row8 = lane >> 3, pos = lane & 7, row0 = r * 128;
    const int xsw = ((w & 1) << 2) | (row8 >> 1);
    #pragma unroll
    for (int q = 0; q < 4; ++q) lo.vx[q] = (unsigned)((row0 + (w + 4 * q) * 8 + row8) * 1024 + ((pos ^ xsw) << 4));
    const int hr = 4 * w + (row8 & 3);
    const int hgr = row0 >= 16 ? row0 - 16 + hr : hr;
    lo.vxh = (unsigned)(hgr * 1024 + ((pos ^ ((hr >> 1) & 7)) << 4));
    const int wf = ((row8 >> 1) & 1) | ((w & 1) << 1);
    lo.wch = (((pos >> 1) ^ wf) << 1) | (pos & 1);
    #pragma unroll
    for (int i = 0; i < 6; ++i) { int ii = w + 4 * i; lo.vw[i] = (unsigned)(((ii & 7) * 8 + row8) * 3072 + (ii >> 3) * 1024 + Q * 128 + lo.wch * 16); }
    return lo;
}
DI void loader(const u16* x, const u16* wqa, const u16* wqb, const u16* woa, const u16* wob, const u16* gw, char* ws, unsigned lds0,
               bool tb, int r, int Q, int rh2, int c2, int w, int lane, unsigned& tag) {
    const LoaderOffs lo = loader_offsets(r, Q, w, lane);
    const int row8 = lane >> 3, row0 = r * 128;
    const unsigned (&vx)[4] = lo.vx; const unsigned (&vw)[6] = lo.vw; const unsigned vxh = lo.vxh; const int wch = lo.wch;
    const u16* W = tb ? wqb : wqa;
    auto issue = [&](int g, int buf, int n) {
        const unsigned lb = lds0 + buf * STAGE;
        if (n < 6) glds((const char*)W + (size_t)g * 64 * 3072, vw[n], lb + XB + (w + 4 * n) * 1024);
        else glds((const char*)x + g * 128, vx[n - 6], lb + 2048 + (w + 4 * (n - 6)) * 1024);
    };
    auto issue_group = [&](int g, int buf) {
        if (tb) glds_half((const char*)x + g * 128, vxh, lds0 + buf * STAGE + w * 512);
        #pragma unroll
        for (int n = 0; n < 10; ++n) issue(g, buf, n);
    };
    auto wait_but = [&](auto extra_c) {
        constexpr int EXTRA = decltype(extra_c)::value;
        if (tb) waitv<11 + EXTRA>(); else waitv<10 + EXTRA>();
    };
    issue_group(0, 0);
    if (tb) glds_half((const char*)x + 128, vxh, lds0 + STAGE + w * 512);
    #pragma unroll
    for (int n = 0; n < 5; ++n) issue(1, 1, n);
    waitv<5>();
    BAR();
    #pragma unroll
    for (int n = 5; n < 10; ++n) issue(1, 1, n);
    issue_group(2, 2);
    u32x4 gx0, gx1, gwv; unsigned epoch;
    {
        const int grow = row0 + ((tb ? 8 : 0) + Q) * 8 + 2 * w;
        unsigned vo = (unsigned)(grow * 1024 + lane * 16), vg = (unsigned)(lane * 16);
        asm volatile("global_load_dwordx4 %0, %1, %2" : "=&v"(gx0) : "v"(vo), "s"(x) : "memory");
        asm volatile("global_load_dwordx4 %0, %1, %2 offset:1024" : "=&v"(gx1) : "v"(vo), "s"(x) : "memory");
        asm volatile("global_load_dwordx4 %0, %1, %2" : "=&v"(gwv) : "v"(vg), "s"(gw) : "memory");
        asm volatile("global_load_dword %0, %1, %2 sc1" : "=&v"(epoch) : "v"(0u), "s"(ws) : "memory");
    }
    const unsigned vwo = (unsigned)((w * 8 + row8) * 1024 + c2 * 128 + wch * 16);
    auto issue_wo = [&](const u16* wo, int ldsoff) {
        #pragma unroll
        for (int i = 0; i < 16; ++i) glds((const char*)wo + i * 32768, vwo, lds0 + ldsoff + (w + 4 * i) * 1024);
    };
    wait_but(IC<4>{});
    BAR();
    issue_group(3, 0);
    wait_but(IC<0>{});
    asm volatile("" : "+v"(gx0), "+v"(gx1), "+v"(gwv), "+v"(epoch));
    tag = __builtin_amdgcn_readfirstlane(epoch) + 1u;
    BAR();
    issue_group(4, 1);
    {
        float d0 = 0.f, d1 = 0.f;
        #pragma unroll
        for (int i = 0; i < 4; ++i) {
            float wl = __uint_as_float(gwv[i] << 16), wh = __uint_as_float(gwv[i] & 0xffff0000u);
            d0 += __uint_as_float(gx0[i] << 16) * wl + __uint_as_float(gx0[i] & 0xffff0000u) * wh;
            d1 += __uint_as_float(gx1[i] << 16) * wl + __uint_as_float(gx1[i] & 0xffff0000u) * wh;
        }
        d0 = xrow_sum(row_sum16(d0)); d1 = xrow_sum(row_sum16(d1));
        const float d = lane == 0 ? d0 : d1;
        const float gsel = __builtin_amdgcn_rcpf(1.f + __builtin_amdgcn_exp2f(-1.4426950408889634f * d));
        if (lane < 2) *(LAS float*)(size_t)(lds0 + LDS_GATE + (2 * w + lane) * 4) = gsel;
        waitl0();
    }
    int buf = 2;
    #pragma unroll 1
    for (int i = 2; i < 5; ++i) {
        wait_but(IC<0>{});
        BAR();
        issue_group(i + 3, buf);
        buf = buf == 2 ? 0 : buf + 1;
    }
    wait_but(IC<0>{}); BAR();
    issue_wo(wob, WO_B);
    waitv<16>(); BAR();
    waitv<0>(); BAR();
    if (!tb) lds_wait<1>(lds0 + LDS_CNT + CNT_PS * 4, 4u);
    issue_wo(woa, WO_A);
    if (w == 0 && lane < 8) {
        const u32x4 gv = *(const LAS u32x4*)(size_t)(lds0 + LDS_GATE + (lane & 1) * 16);
        asm volatile("global_store_dwordx4 %0, %1, off sc1\n\ts_nop 1" :: "v"(ws + WS_G + (r * 16 + (tb ? 8 : 0) + Q) * 128 + lane * 16), "v"(gv) : "memory");
    }
    waitv<0>();
    if (lane == 0) lds_inc(lds0 + LDS_CNT + CNT_WO * 4);
    const int g = lane >> 4, s = lane & 15, q4 = s >> 2, p4 = lane & 3;
    const int rf = ((q4 >> 1) & 1) | ((g & 1) << 1);
    const __amdgpu_buffer_rsrc_t rws = __builtin_amdgcn_make_buffer_rsrc(ws, 0, WS_END, 0x00020000);
    OutHalf oh;
    oh.pre(lds0 + WO_B + (8 * g + q4) * 128 + p4 * 8, rf);
    f32x4 ob[4];
    #pragma unroll
    for (int n = 0; n < 4; ++n) ob[n] = (f32x4){0, 0, 0, 0};
    if (w == 0) {
        wait_flags<4>(ws + WS_F2 + ((rh2 >> 1) * 16 + 8 + (lane & 7)) * 128, tag);
        if (lane == 0) lds_inc(lds0 + LDS_CNT + CNT_BF * 4);
    } else lds_wait<2>(lds0 + LDS_CNT + CNT_BF * 4, 1u);
    oh.run(rws, WS_ACT + (128 + rh2 * 4 + w) * 16384, lane, ob);
    #pragma unroll
    for (int n = 0; n < 4; ++n) *(LAS f32x4*)(size_t)(lds0 + LDS_RESB + w * 4096 + n * 1024 + lane * 16) = ob[n];
    waitl0();
    if (lane == 0) lds_inc(lds0 + LDS_CNT + (CNT_RES + w) * 4);
}

template <int NM> struct Frags { bf16x8 x[NM]; s16x4 wl[3], wh[3]; };

template <bool TB>
DI void matrix(u16* out, char* ws, unsigned lds0, int r, int Q, int rh2, int c2, int w, int lane) {
    constexpr int NM = TB ? 9 : 8;
    constexpr int NMF = TB ? 26 : 24;
    constexpr int NRD = NM + 6;
    constexpr int RD0 = 2;
    const unsigned tag = __hip_atomic_load((gu32*)(ws + WS_EPOCH), RLX_AGENT) + 1u;
    const int g = lane >> 4, s = lane & 15;
    const unsigned ax0 = (unsigned)((TB ? 0 : 2048) + s * 128 + ((g ^ (s >> 1)) << 4));
    const int q4 = s >> 2, p4 = lane & 3;
    const int rf = ((q4 >> 1) & 1) | ((g & 1) << 1);
    const unsigned awl = (unsigned)((8 * g + q4) * 128 + p4 * 8);
    f32x4 accq[NM], acck[NM], accv[NM];
    #pragma unroll
    for (int i = 0; i < NM; ++i) { accq[i] = (f32x4){0, 0, 0, 0}; acck[i] = (f32x4){0, 0, 0, 0}; accv[i] = (f32x4){0, 0, 0, 0}; }
    #pragma unroll
    for (int i = 0; i < NM; ++i) { if (!TB || i > 0) asm volatile("" : "+v"(accq[i])); asm volatile("" : "+v"(acck[i]), "+v"(accv[i])); }
    if (w == 0 && lane < 16) *(lu32*)(size_t)(lds0 + LDS_CNT + lane * 4) = 0u;
    waitl0();
    Frags<NM> A, B;
    auto rd_n = [&](Frags<NM>& F, unsigned bwv, unsigned bxv, int n) {
        if (n < 0 || n >= NRD) return;
        if (n < 6) { s16x4 v = trread(bwv + (n >> 1) * 8192 + (n & 1) * 512); if (n & 1) F.wh[n >> 1] = v; else F.wl[n >> 1] = v; }
        else F.x[n - 6] = *(const LAS bf16x8*)(size_t)(bxv + (n - 6) * 2048);
    };
    auto mm = [&](const Frags<NM>& F, int n) {
        int mu, t;
        if (TB) { if (n < 2) { mu = 0; t = n + 1; } else { mu = 1 + (n - 2) / 3; t = (n - 2) % 3; } } else { mu = n / 3; t = n % 3; }
        if (t == 0) accq[mu] = mfma32(cat8(F.wl[0], F.wh[0]), F.x[mu], accq[mu]);
        else if (t == 1) { if (TB) acck[mu] = mfma32(cat8(F.wl[1], F.wh[1]), F.x[mu], acck[mu]); else acck[mu] = mfma32(F.x[mu], cat8(F.wl[1], F.wh[1]), acck[mu]); }
        else accv[mu] = mfma32(F.x[mu], cat8(F.wl[2], F.wh[2]), accv[mu]);
    };
    BAR();
    unsigned bx0 = lds0 + ax0, bx1 = lds0 + (ax0 ^ 64), bw = lds0 + XB + awl + ((w ^ rf) << 5);
    #pragma unroll
    for (int n = 0; n < NRD; ++n) rd_n(A, bw, bx0, n);
    int buf = 0;
    auto stage = [&](auto last_c) {
        constexpr int LAST = decltype(last_c)::value;
        SB();
        #pragma unroll
        for (int n = 0; n < NMF; ++n) { mm(A, n); rd_n(B, bw + 4096, bx1, n - RD0); SB(); }
        const int nbuf = buf == 2 ? 0 : buf + 1;
        const unsigned d = (unsigned)((nbuf - buf) * STAGE);
        bx0 += d; bx1 += d; bw += d; buf = nbuf;
        waitl0();
        BAR();
        SB();
        if constexpr (LAST && !TB) {
            #pragma unroll
            for (int mu = 0; mu < 8; ++mu) mm(B, mu * 3 + 1);
            SB();
            #pragma unroll
            for (int mu = 0; mu < 8; ++mu) mm(B, mu * 3 + 2);
            #pragma unroll
            for (int mu = 0; mu < 8; ++mu) mm(B, mu * 3);
        } else {
            #pragma unroll
            for (int n = 0; n < NMF; ++n) { mm(B, n); if (!LAST) rd_n(A, bw, bx0, n - RD0); SB(); }
        }
    };
    #pragma unroll 1
    for (int i = 0; i < 7; ++i) stage(IC<0>{});
    stage(IC<1>{});

    const __amdgpu_buffer_rsrc_t rws = __builtin_amdgcn_make_buffer_rsrc(ws, 0, WS_END, 0x00020000);
    const int head = Q * 4 + w;
    s16x4 op[8];
    if (!TB) {
        f32x4 kv = {0, 0, 0, 0}, z = {0, 0, 0, 0};
        const bf16x8 ones = __builtin_bit_cast(bf16x8, (u32x4){0x3f803f80u, 0x3f803f80u, 0x3f803f80u, 0x3f803f80u});
        #pragma unroll
        for (int u = 0; u < 4; ++u) {
            bf16x8 pk = cat8(phi4(acck[2 * u]), phi4(acck[2 * u + 1]));
            bf16x8 vv = cat8(pk4(accv[2 * u]), pk4(accv[2 * u + 1]));
            kv = mfma32(pk, vv, kv);
            z = mfma32(pk, ones, z);
        }
        const int pbase = __builtin_amdgcn_readfirstlane(WS_P1 + (head * 16 + r) * P1_STRIDE);
        __builtin_amdgcn_raw_buffer_store_b64(__builtin_bit_cast(u32x2, pk4(kv)), rws, lane * 8, pbase, 16);
        if (s < 4) __builtin_amdgcn_raw_buffer_store_b64(__builtin_bit_cast(u32x2, pk4(z)), rws, 512 + (s * 4 + g) * 8, pbase, 16);
        if (lane == 0) lds_inc(lds0 + LDS_CNT + CNT_PS * 4);
        SB();
        s16x4 pq[8];
        #pragma unroll
        for (int t = 0; t < 4; ++t) { pq[t] = phi4(accq[t]); asm volatile("" : "+v"(pq[t])); }
        waitv<0>();
        if (lane == 0) flag_set(ws + WS_F1 + (head * 16 + r) * 128, tag);
        #pragma unroll
        for (int t = 4; t < 8; ++t) { pq[t] = phi4(accq[t]); asm volatile("" : "+v"(pq[t])); }
        const int dg = s - 4 * g;
        s16x4 ident = {(short)(dg == 0 ? 0x3f80 : 0), (short)(dg == 1 ? 0x3f80 : 0), (short)(dg == 2 ? 0x3f80 : 0), (short)(dg == 3 ? 0x3f80 : 0)};
        asm volatile("" : "+v"(ident));
        wait_flags<1>(ws + WS_F1 + (head * 16 + s) * 128, tag);
        u32x2 part[16];
        const int pb0 = __builtin_amdgcn_readfirstlane(WS_P1 + head * 16 * P1_STRIDE);
        const u32x2 zp = __builtin_amdgcn_raw_buffer_load_b64(rws, s * P1_STRIDE + 512 + g * 8, pb0, 16);
        #pragma unroll
        for (int i = 0; i < 16; ++i) part[i] = __builtin_amdgcn_raw_buffer_load_b64(rws, lane * 8, pb0 + i * P1_STRIDE, 16);
        SB();
        f32x4 zt = {__uint_as_float(zp[0] << 16), __uint_as_float(zp[0] & 0xffff0000u), __uint_as_float(zp[1] << 16), __uint_as_float(zp[1] & 0xffff0000u)};
        row_sum16x4(zt);
        const s16x4 za = pk4(zt);
        f32x4 den[8];
        #pragma unroll
        for (int t = 0; t < 8; ++t) den[t] = mfma16(za, pq[t], (f32x4){0, 0, 0, 0});
        SB();
        f32x4 k0 = {0, 0, 0, 0}, k1 = {0, 0, 0, 0};
        #pragma unroll
        for (int i = 0; i < 16; i += 2) { k0 = mfma16(ident, __builtin_bit_cast(s16x4, part[i]), k0); k1 = mfma16(ident, __builtin_bit_cast(s16x4, part[i + 1]), k1); }
        float inv[8];
        #pragma unroll
        for (int t = 0; t < 8; ++t) inv[t] = __builtin_amdgcn_rcpf(den[t][0] + 1e-6f);
        const s16x4 kva = pk4(k0 + k1);
        SB();
        f32x4 num[8];
        #pragma unroll
        for (int t = 0; t < 8; ++t) num[t] = mfma16(kva, pq[t], (f32x4){0, 0, 0, 0});
        SB();
        #pragma unroll
        for (int t = 0; t < 8; ++t) op[t] = pk4(num[t] * inv[t]);
    } else {
        const float NEG = -1e30f;
        f32x4 mprev, mcur;
        #pragma unroll
        for (int j = 0; j < 4; ++j) {
            const int dc = s - 4 * g - j;
            mcur[j] = (dc >= 0 && dc <= 14 && (dc & 1) == 0) ? 0.f : NEG;
            mprev[j] = (dc + 16 <= 14 && (dc & 1) == 0) ? 0.f : NEG;
        }
        const f32x4 mfirst = r == 0 ? (f32x4){NEG, NEG, NEG, NEG} : mprev;
        const unsigned pmask = (s & 1) ? 0xffff0000u : 0x0000ffffu;
        const s16x4 ones4 = {0x3f80, 0x3f80, 0x3f80, 0x3f80};
        const float cs = 0.25f * 1.4426950408889634f;
        s16x4 kp[9], vp[9], qp[8];
        #pragma unroll
        for (int i = 0; i < 9; ++i) { kp[i] = pk4(acck[i]); if (i < 8) qp[i] = pk4(accq[i + 1]); }
        f32x4 sp[8], sc[8];
        #pragma unroll
        for (int t = 0; t < 8; ++t) { sp[t] = mfma16(kp[t], qp[t], t == 0 ? mfirst : mprev); sc[t] = mfma16(kp[t + 1], qp[t], mcur); }
        #pragma unroll
        for (int i = 0; i < 9; ++i) vp[i] = pk4(accv[i]);
        SB();
        float a[8][4], mx[8];
        #pragma unroll
        for (int t = 0; t < 8; ++t) {
            a[t][0] = fmx(sp[t][0], sp[t][1]); a[t][1] = fmx(sp[t][2], sp[t][3]);
            a[t][2] = fmx(sc[t][0], sc[t][1]); a[t][3] = fmx(sc[t][2], sc[t][3]);
            mx[t] = fmx(fmx(fmx(a[t][0], a[t][1]), a[t][2]), a[t][3]);
        }
        SB();
        xrow_max4(mx[0], mx[1], mx[2], mx[3]);
        xrow_max4(mx[4], mx[5], mx[6], mx[7]);
        SB();
        s16x4 pp[8], pc[8];
        #pragma unroll
        for (int t = 0; t < 8; ++t) {
            const float nm = -mx[t] * cs;
            float e[4];
            #pragma unroll
            for (int j = 0; j < 4; ++j) e[j] = __builtin_amdgcn_exp2f(__builtin_fmaf(a[t][j], cs, nm));
            pp[t] = __builtin_bit_cast(s16x4, (u32x2){pk2(e[0], e[0]) & pmask, pk2(e[1], e[1]) & pmask});
            pc[t] = __builtin_bit_cast(s16x4, (u32x2){pk2(e[2], e[2]) & pmask, pk2(e[3], e[3]) & pmask});
        }
        SB();
        f32x4 o[8], sm[8];
        #pragma unroll
        for (int t = 0; t < 8; ++t) { sm[t] = mfma16(ones4, pp[t], (f32x4){0, 0, 0, 0}); o[t] = mfma16(vp[t], pp[t], (f32x4){0, 0, 0, 0}); }
        #pragma unroll
        for (int t = 0; t < 8; ++t) { sm[t] = mfma16(ones4, pc[t], sm[t]); o[t] = mfma16(vp[t + 1], pc[t], o[t]); }
        SB();
        float inv[8];
        #pragma unroll
        for (int t = 0; t < 8; ++t) inv[t] = __builtin_amdgcn_rcpf(sm[t][0]);
        #pragma unroll
        for (int t = 0; t < 8; ++t) op[t] = pk4(o[t] * inv[t]);
    }
    {
        const int abase = __builtin_amdgcn_readfirstlane(WS_ACT + (((TB ? 128 : 0) + r * 8) * 16 + (head >> 1)) * 1024 + (head & 1) * 512);
        const int alane = (g & 1) * 16384 + ((g >> 1) * 16 + s) * 16;
        #pragma unroll
        for (int u = 0; u < 4; ++u)
            __builtin_amdgcn_raw_buffer_store_b128(pair16(op[2 * u], op[2 * u + 1]), rws, alane, abase + u * 2 * 16384, 16);
        lds_wait<0>(lds0 + LDS_CNT + CNT_WO * 4, 4u);
        waitv<0>();
        unsigned old = 0;
        if (lane == 0) old = lds_inc(lds0 + LDS_CNT + CNT_PUB * 4);
        old = __builtin_amdgcn_readfirstlane(old);
        if (old == 3u && lane == 0) flag_set(ws + WS_F2 + (r * 16 + (TB ? 8 : 0) + Q) * 128, tag);
    }
    const int mt = rh2 * 4 + w, r2 = rh2 >> 1;
    OutHalf oh;
    oh.pre(lds0 + WO_A + awl, rf);
    f32x4 oa[4];
    #pragma unroll
    for (int n = 0; n < 4; ++n) oa[n] = (f32x4){0, 0, 0, 0};
    if (w == 0) {
        wait_flags<1>(ws + WS_F2 + (r2 * 16 + (lane & 7)) * 128, tag);
        if (lane == 0) lds_inc(lds0 + LDS_CNT + CNT_AF * 4);
    } else lds_wait<1>(lds0 + LDS_CNT + CNT_AF * 4, 1u);
    const float gt = __uint_as_float(__hip_atomic_load((gu32*)(ws + WS_G) + (r2 * 16 + (mt & 7) * 2 + (s >> 3)) * 32 + (s & 7), RLX_AGENT));
    oh.run(rws, WS_ACT + mt * 16384, lane, oa);
    lds_wait<0>(lds0 + LDS_CNT + (CNT_RES + w) * 4, 1u);
    s16x4 res[4];
    #pragma unroll
    for (int n = 0; n < 4; ++n) {
        const f32x4 ob = *(const LAS f32x4*)(size_t)(lds0 + LDS_RESB + w * 4096 + n * 1024 + lane * 16);
        res[n] = pk4(oa[n] * gt + ob * (1.f - gt));
    }
    {
        const __amdgpu_buffer_rsrc_t ro = __builtin_amdgcn_make_buffer_rsrc(out, 0, 2048 * 1024, 0x00020000);
        const u32x4 p0 = pair16(res[0], res[1]), p1 = pair16(res[2], res[3]);
        u32x4 da, db;
        #pragma unroll
        for (int i = 0; i < 4; ++i) {
            const unsigned q = (unsigned)__builtin_amdgcn_mov_dpp((int)p1[i], 0x128, 0xf, 0xf, true);
            da[i] = s < 8 ? p0[i] : q; db[i] = s < 8 ? q : p0[i];
        }
        const int ch = (g & 1) * 32 + (g >> 1) * 16;
        const int va = (mt * 16 + (s & 7)) * 1024 + c2 * 128 + ch + (s < 8 ? 0 : 64);
        const int vb = (mt * 16 + 8 + (s & 7)) * 1024 + c2 * 128 + ch + (s < 8 ? 64 : 0);
        __builtin_amdgcn_raw_buffer_store_b128(da, ro, va, 0, 17);
        __builtin_amdgcn_raw_buffer_store_b128(db, ro, vb, 0, 17);
    }
}

__global__ __launch_bounds__(512, 2) void fused_kernel(const u16* __restrict__ x, const u16* __restrict__ wqa, const u16* __restrict__ woa,
                                                       const u16* __restrict__ wqb, const u16* __restrict__ wob, const u16* __restrict__ gw,
                                                       u16* __restrict__ out, char* __restrict__ ws) {
    __shared__ __attribute__((aligned(16))) char smem[163840];
    const int lane = threadIdx.x & 63;
    const int wave = __builtin_amdgcn_readfirstlane(threadIdx.x >> 6);
    const int w = wave & 3;
    const int b = blockIdx.x, xcd = b & 7, j = b >> 3;
    const int r = (xcd >> 2) * 8 + (j >> 2);
    const bool tb = (j >> 1) & 1;
    const int Q = (xcd & 3) * 2 + (j & 1);
    const int rh2 = xcd * 4 + (j >> 3), c2 = j & 7;
    const unsigned lds0 = (unsigned)(size_t)(LAS char*)smem;
    if (__builtin_expect(wave >= 4, 1)) {
        unsigned tag = 0;
        loader(x, wqa, wqb, woa, wob, gw, ws, lds0, tb, r, Q, rh2, c2, w, lane, tag);
        if (b == 0 && wave == 4) {
            #pragma unroll 1
            for (int i = 0; i < 4; ++i) wait_flags<8>(ws + WS_F2 + (i * 64 + lane) * 128, tag);
            if (lane == 0) flag_set(ws + WS_EPOCH, tag);
        }
        return;
    }
    {
        const int l48 = lane < 48 ? lane : 0;
        if (w == 0) glds4s(x, (unsigned)((b * 64 + lane) * 128), lds0 + LDS_JUNK);
        else if (w == 1) glds4s(wqa, (unsigned)((b * 48 + l48) * 128), lds0 + LDS_JUNK);
        else if (w == 2) glds4s(wqb, (unsigned)((b * 48 + l48) * 128), lds0 + LDS_JUNK);
        else glds4((const char*)(lane < 32 ? woa : wob) + (b * 16 + (lane & 15)) * 128, lds0 + LDS_JUNK);
    }
    if (tb) matrix<true>(out, ws, lds0, r, Q, rh2, c2, w, lane);
    else matrix<false>(out, ws, lds0, r, Q, rh2, c2, w, lane);
}

extern "C" void kernel_launch(void* const* d_in, const int* in_sizes, int n_in, void* d_out, int out_size, void* d_ws, size_t ws_size,
                              hipStream_t stream) {
    fused_kernel<<<256, 512, 0, stream>>>((const u16*)d_in[0], (const u16*)d_in[1], (const u16*)d_in[2], (const u16*)d_in[4],
                                          (const u16*)d_in[5], (const u16*)d_in[6], (u16*)d_out, (char*)d_ws);
}
```
